```python
import torch

def _softmax(x, axis=-1):
    return torch.softmax(x, dim=axis)
def _gelu(x):
    return torch.nn.functional.gelu(x, approximate="tanh")
def _silu(x):
    return torch.nn.functional.silu(x)
def _sigmoid(x):
    return torch.sigmoid(x)
def _layernorm(x, axis=-1, eps=1e-5):
    m = x.mean(dim=axis, keepdim=True)
    v = x.var(dim=axis, keepdim=True, unbiased=False)
    return (x - m) / torch.sqrt(v + eps)
def _scatter_add(tgt, idx, src, axis=0):
    return tgt.clone().index_add_(axis, idx, src)
def _segment_sum(src, seg, n):
    out = torch.zeros((n,) + src.shape[1:], dtype=src.dtype)
    return out.index_add_(0, seg, src)
def _pad(x, pw):
    flat = tuple(v for pair in reversed(pw) for v in pair)
    return torch.nn.functional.pad(x, flat)

kernel_name = 'gated_mix__spectral_fnet__mha_block__prompt'

def setup_inputs():

    _w = (lambda a: (lambda t: t.to(torch.bfloat16) if t.is_floating_point() else t)(torch.as_tensor(a)))
    def _A_setup():
        B, S, D = 1, 2048, 640
        ns = torch.arange(S)[:, None] * torch.arange(S)[None, :]
        nd = torch.arange(D)[:, None] * torch.arange(D)[None, :]
        Ws_re = torch.cos(-2.0 * torch.pi * ns / S).to(float)
        Ws_im = torch.sin(-2.0 * torch.pi * ns / S).to(float)
        Wd_re = torch.cos(-2.0 * torch.pi * nd / D).to(float)
        Wd_im = torch.sin(-2.0 * torch.pi * nd / D).to(float)
        return {
            "x": _w((lambda *s: torch.randn(*s, dtype=torch.bfloat16) * 0.05)(B, S, D)),
            "dft_seq_re": _w(Ws_re),
            "dft_seq_im": _w(Ws_im),
            "dft_hid_re": _w(Wd_re),
            "dft_hid_im": _w(Wd_im),
        }
    _A_in = _A_setup()

    def _B_setup():
        B, S, D, H = 1, 2048, 640, 32
        return {
            "x": _w((lambda *s: torch.randn(*s, dtype=torch.bfloat16) * 0.05)(B, S, D)),
            "wqkv": _w((lambda *s: torch.randn(*s, dtype=torch.bfloat16) * 1.6)(D, 3 * D)),
            "wo": _w((lambda *s: torch.randn(*s, dtype=torch.bfloat16) * 1.6)(D, D)),
        }
    _B_in = _B_setup()

    return {
        "A_x": _A_in["x"],
        "A_dft_seq_re": _A_in["dft_seq_re"],
        "A_dft_seq_im": _A_in["dft_seq_im"],
        "A_dft_hid_re": _A_in["dft_hid_re"],
        "A_dft_hid_im": _A_in["dft_hid_im"],
        "B_x": _B_in["x"],
        "B_wqkv": _B_in["wqkv"],
        "B_wo": _B_in["wo"],
        "gate_w": _w((lambda *s: torch.randn(*s, dtype=torch.bfloat16) * 0.05)(640) * 16.0),
    }

def reference(A_x, A_dft_seq_re, A_dft_seq_im, A_dft_hid_re, A_dft_hid_im, B_x, B_wqkv, B_wo, gate_w):
    def _A_ref(x, dft_seq_re, dft_seq_im, dft_hid_re, dft_hid_im):
        a = torch.einsum("bsd,ed->bse", x, dft_hid_re)
        b = torch.einsum("bsd,ed->bse", x, dft_hid_im)
        y = torch.einsum("ts,bse->bte", dft_seq_re, a) - torch.einsum("ts,bse->bte", dft_seq_im, b)
        return y

    def _B_ref(x, wqkv, wo):
        B, S, D = x.shape
        H = 32
        Dh = D // H
        qkv = (lambda x, *d: x.permute(*d))((x @ wqkv).reshape(B, S, 3, H, Dh), 2, 0, 3, 1, 4)
        q, k, v = qkv[0], qkv[1], qkv[2]
        scores = torch.einsum("bhqd,bhkd->bhqk", q, k) * (Dh ** -0.5)
        p = _softmax(scores, axis=-1)
        o = torch.einsum("bhqk,bhkd->bhqd", p, v)
        return (lambda x, *d: x.permute(*d))(o, 0, 2, 1, 3).reshape(B, S, D) @ wo

    x = A_x
    g = _sigmoid((x * gate_w).sum(-1, keepdims=True))
    a = _A_ref(x, A_dft_seq_re, A_dft_seq_im, A_dft_hid_re, A_dft_hid_im)
    b = _B_ref(x, B_wqkv, B_wo)
    return g * a + (1 - g) * b
```

```cpp
#include <hip/hip_runtime.h>
#include <hip/hip_bf16.h>
#include <stdint.h>

typedef short bf16x8 __attribute__((ext_vector_type(8)));
typedef short s16x4 __attribute__((ext_vector_type(4)));
typedef float f32x16 __attribute__((ext_vector_type(16)));
typedef float f32x4 __attribute__((ext_vector_type(4)));
typedef float f32x2 __attribute__((ext_vector_type(2)));
typedef unsigned u32x4 __attribute__((ext_vector_type(4)));
typedef unsigned u32x2 __attribute__((ext_vector_type(2)));
typedef __bf16 bf16x2_t __attribute__((ext_vector_type(2)));
#define LDSP __attribute__((address_space(3)))
#define DI __device__ __forceinline__
typedef __amdgpu_buffer_rsrc_t rsrc_t;

constexpr int S_ = 2048, D_ = 640, H_ = 32;
constexpr unsigned Q_HEAD = 64 * 1280;
constexpr unsigned KV_CHUNK = 22528;
constexpr unsigned KV_HEAD = 8 * KV_CHUNK;
constexpr unsigned OFF_Q = 0;
constexpr unsigned OFF_KV = OFF_Q + H_ * Q_HEAD;
constexpr unsigned OFF_AB = OFF_KV + H_ * KV_HEAD;
constexpr unsigned OFF_Y = OFF_AB + 4 * 64 * 20480;
constexpr unsigned OFF_O = OFF_Y + 4 * S_ * D_ * 2;
constexpr unsigned OFF_WOT = OFF_O + S_ * D_ * 2;
constexpr unsigned OFF_G = OFF_WOT + D_ * D_ * 2;
constexpr unsigned WS_BYTES = OFF_G + S_ * 4;

#define MFMA32(a, b, c) __builtin_amdgcn_mfma_f32_32x32x16_bf16((a), (b), (c), 0, 0, 0)
#define MFMA16(a, b, c) __builtin_amdgcn_mfma_f32_16x16x32_bf16((a), (b), (c), 0, 0, 0)

DI unsigned pk(float a, float b) { f32x2 v = {a, b}; return __builtin_bit_cast(unsigned, __builtin_convertvector(v, bf16x2_t)); }
DI float bf_lo(unsigned u) { return __uint_as_float(u << 16); }
DI float bf_hi(unsigned u) { return __uint_as_float(u & 0xffff0000u); }
DI rsrc_t mk_rsrc(const void* p, unsigned bytes) { return __builtin_amdgcn_make_buffer_rsrc((void*)p, (short)0, (int)bytes, 0x00020000); }
DI void dma16(unsigned lds_addr, unsigned voff, rsrc_t rs, unsigned soff) {
    asm volatile("s_mov_b32 m0, %0\n\ts_nop 0\n\tbuffer_load_dwordx4 %1, %2, %3 offen lds" ::"s"(lds_addr), "v"(voff), "s"(rs), "s"(soff) : "memory");
}
#define WAIT_VM(n) asm volatile("s_waitcnt vmcnt(" #n ")" ::: "memory")
#define BAR() asm volatile("s_barrier" ::: "memory")
DI bf16x8 lds_rd16(unsigned a) { return *(const LDSP bf16x8*)(a); }
DI s16x4 lds_tr(unsigned a) { return __builtin_amdgcn_ds_read_tr16_b64_v4i16((LDSP s16x4*)(a)); }
DI int swap23(int i) { return (i & ~12) | ((i & 4) << 1) | ((i & 8) >> 1); }

constexpr int K1_XB = 208 * 128, K1_STAGE = K1_XB + 128 * 128, K1_LDS = 3 * K1_STAGE;

template <int KIND>
DI void k1_body(const __hip_bfloat16* x, const __hip_bfloat16* w, unsigned wbytes, int wcol0, int m0, int nt, char* ws, unsigned lds0) {
    const int tid = threadIdx.x, l = tid & 63;
    const int wv = __builtin_amdgcn_readfirstlane(tid >> 6);
    const int wn = wv & 3, wm = wv >> 2;
    const rsrc_t rx = mk_rsrc(x, S_ * D_ * 2), rw = mk_rsrc(w, wbytes);
    unsigned xoff[4];
#pragma unroll
    for (int jj = 0; jj < 4; ++jj) {
        int rho = 8 * (wv + 8 * jj) + (l >> 3);
        if (rho > 207) rho = 207;
        int s = rho;
        if (KIND != 1 && rho < 192) { const int i = rho & 15; s = (rho & ~31) + 8 * (i >> 2) + 4 * ((rho >> 4) & 1) + (i & 3); }
        xoff[jj] = (unsigned)((m0 + s) * (D_ * 2) + (((l & 7) ^ (l >> 3)) << 4));
    }
    unsigned woff; int wjstep, wkstep;
    if (KIND == 0) {
        woff = (unsigned)((wcol0 + 8 * wv + (l >> 3)) * (D_ * 2) + (((l & 7) ^ (l >> 3)) << 4));
        wjstep = 64 * D_ * 2; wkstep = 128;
    } else {
        const int k = 4 * wv + (l >> 4), p = l & 15;
        const int f = (k & 3) | (((k >> 3) & 1) << 2);
        woff = (unsigned)(k * (1920 * 2) + wcol0 * 2 + (((p >> 1) ^ f) << 5) + ((p & 1) << 4));
        wjstep = 32 * 1920 * 2; wkstep = 64 * 1920 * 2;
    }
    auto stage = [&](int kt) {
        const unsigned base = lds0 + (kt % 3) * K1_STAGE + wv * 1024;
        dma16(base, xoff[0], rx, kt * 128);
        dma16(base + 8192, xoff[1], rx, kt * 128);
        dma16(base + 16384, xoff[2], rx, kt * 128);
        if (wv < 2) dma16(base + 24576, xoff[3], rx, kt * 128);
        dma16(base + K1_XB, woff, rw, kt * wkstep);
        dma16(base + K1_XB + 8192, woff, rw, kt * wkstep + wjstep);
    };
    stage(0); stage(1);
    const int i16 = l & 15, g = l >> 4;
    const unsigned xrd0 = lds0 + (wm * 96 + i16) * 128 + ((g ^ (i16 & 7)) << 4), xrd1 = xrd0 ^ 64;
    unsigned wrd[2];
    if (KIND == 0) { wrd[0] = lds0 + K1_XB + (wn * 32 + i16) * 128 + ((g ^ (i16 & 7)) << 4); wrd[1] = wrd[0] ^ 64; }
    else {
        const int q = i16 >> 2, p = l & 3, f = q | ((g & 1) << 2);
        wrd[0] = lds0 + K1_XB + (8 * g + q) * 256 + (((2 * wn) ^ f) << 5) + 8 * p; wrd[1] = wrd[0] ^ 32;
    }
    f32x4 acc[7][2];
#pragma unroll
    for (int m = 0; m < 7; ++m) { acc[m][0] = f32x4{0.f, 0.f, 0.f, 0.f}; acc[m][1] = f32x4{0.f, 0.f, 0.f, 0.f}; }
#pragma unroll
    for (int kt = 0; kt < 10; ++kt) {
        if (kt < 9) { if (wv < 2) WAIT_VM(6); else WAIT_VM(5); } else WAIT_VM(0);
        BAR();
        if (kt + 2 < 10) stage(kt + 2);
        const unsigned so = (kt % 3) * K1_STAGE;
#pragma unroll
        for (int kk = 0; kk < 2; ++kk) {
            bf16x8 wf[2];
#pragma unroll
            for (int j = 0; j < 2; ++j) {
                if (KIND == 0) wf[j] = lds_rd16(wrd[kk] + so + j * 2048);
                else {
                    const s16x4 lo = lds_tr(wrd[j] + so + kk * 8192), hi = lds_tr(wrd[j] + so + kk * 8192 + 1024);
                    wf[j] = __builtin_shufflevector(lo, hi, 0, 1, 2, 3, 4, 5, 6, 7);
                }
            }
            const unsigned xr = (kk ? xrd1 : xrd0) + so;
#pragma unroll
            for (int m = 0; m < 6; ++m) {
                const bf16x8 xf = lds_rd16(xr + m * 2048);
#pragma unroll
                for (int j = 0; j < 2; ++j) acc[m][j] = (KIND == 1) ? MFMA16(wf[j], xf, acc[m][j]) : MFMA16(xf, wf[j], acc[m][j]);
            }
            if (wm == 0) {
                const bf16x8 xf = lds_rd16(xr + 12 * 2048);
#pragma unroll
                for (int j = 0; j < 2; ++j) acc[6][j] = (KIND == 1) ? MFMA16(wf[j], xf, acc[6][j]) : MFMA16(xf, wf[j], acc[6][j]);
            }
        }
    }
    if (KIND == 0) {
        const bool isb = nt >= 5;
        const float sg = isb ? -1.f : 1.f;
#pragma unroll
        for (int j = 0; j < 2; ++j) {
            const int e = wcol0 + wn * 32 + 16 * j + i16;
            const int ntile = e / 160, er = e - ntile * 160;
            char* base = ws + OFF_AB + (unsigned)(ntile * 64 * 20480 + (er >> 5) * 1024 + swap23(er & 31) * 16);
#pragma unroll
            for (int p = 0; p < 3; ++p) {
                const int kg = (isb ? 2048 : 0) + m0 + 32 * (wm * 3 + p) + 8 * g;
                const f32x4 a0 = acc[2 * p][j] * sg, a1 = acc[2 * p + 1][j] * sg;
                const u32x4 v = {pk(a0[0], a0[1]), pk(a0[2], a0[3]), pk(a1[0], a1[1]), pk(a1[2], a1[3])};
                *(u32x4*)(base + (kg >> 4) * 5120 + ((kg >> 3) & 1) * 512) = v;
            }
            if (wm == 0) {
                const int kg = (isb ? 2048 : 0) + m0 + 192 + 4 * g;
                const f32x4 a0 = acc[6][j] * sg;
                const u32x2 v = {pk(a0[0], a0[1]), pk(a0[2], a0[3])};
                *(u32x2*)(base + (kg >> 4) * 5120 + ((kg >> 3) & 1) * 512 + (kg & 4) * 2) = v;
            }
        }
    } else if (KIND == 2) {
#pragma unroll
        for (int j = 0; j < 2; ++j) {
            const int col = wcol0 - 1280 + wn * 32 + 16 * j + i16;
            const int h = col / 20, d = col - h * 20;
            char* base = ws + OFF_KV + (unsigned)(h * KV_HEAD + 10240 + d * 16);
            const u32x4 ones = {0x3F803F80u, 0x3F803F80u, 0x3F803F80u, 0x3F803F80u};
#pragma unroll
            for (int p = 0; p < 3; ++p) {
                const int s = m0 + 32 * (wm * 3 + p) + 8 * g;
                const f32x4 a0 = acc[2 * p][j], a1 = acc[2 * p + 1][j];
                const u32x4 v = {pk(a0[0], a0[1]), pk(a0[2], a0[3]), pk(a1[0], a1[1]), pk(a1[2], a1[3])};
                char* dst = base + (s >> 8) * KV_CHUNK + ((s & 255) >> 3) * 384;
                *(u32x4*)dst = v;
                if (d == 19) *(u32x4*)(dst + 16) = ones;
            }
            if (wm == 0) {
                const int s = m0 + 192 + 4 * g;
                const f32x4 a0 = acc[6][j];
                const u32x2 v = {pk(a0[0], a0[1]), pk(a0[2], a0[3])};
                char* dst = base + (s >> 8) * KV_CHUNK + ((s & 255) >> 3) * 384 + (s & 4) * 2;
                *(u32x2*)dst = v;
                if (d == 19) *(u32x2*)(dst + 16) = u32x2{0x3F803F80u, 0x3F803F80u};
            }
        }
    } else {
        const bool isq = nt < 15;
        const float sc = isq ? 0.22360679774997896f * 1.4426950408889634f : 1.f;
#pragma unroll
        for (int j = 0; j < 2; ++j) {
            const int col = wcol0 - (isq ? 0 : 640) + wn * 32 + 16 * j + 4 * g;
            const int h = col / 20, d0 = col - h * 20;
#pragma unroll
            for (int m = 0; m < 7; ++m) {
                if (m == 6 && wm != 0) break;
                const int s = m0 + (m == 6 ? 192 : wm * 96 + 16 * m) + i16;
                const f32x4 a0 = acc[m][j] * sc;
                const u32x2 v = {pk(a0[0], a0[1]), pk(a0[2], a0[3])};
                unsigned off;
                if (isq) {
                    off = OFF_Q + h * Q_HEAD + (s >> 5) * 1280 + (d0 < 16 ? ((d0 >> 3) * 32 + (s & 31)) * 16 + (d0 & 4) * 2 : 1024 + (s & 31) * 8);
                } else {
                    const int i = swap23(s & 31), T = (s >> 5) & 7;
                    off = OFF_KV + h * KV_HEAD + (s >> 8) * KV_CHUNK +
                          (d0 < 16 ? T * 1024 + ((d0 >> 3) * 32 + i) * 16 + (d0 & 4) * 2 : 8192 + (T >> 1) * 512 + i * 16 + (T & 1) * 8);
                }
                *(u32x2*)(ws + off) = v;
            }
        }
    }
}

__global__ __launch_bounds__(512, 2) void k1_proj(const __hip_bfloat16* x, const __hip_bfloat16* wr, const __hip_bfloat16* wi,
                                                  const __hip_bfloat16* wqkv, char* ws) {
    extern __shared__ __attribute__((aligned(16))) char smem[];
    const unsigned lds0 = (unsigned)(uintptr_t)(LDSP char*)smem;
    const int b = blockIdx.x, xcd = b & 7, slot = b >> 3;
    const int tau = (xcd < 2 ? xcd * 32 : 64 + (xcd - 2) * 31) + slot;
    const int mh = tau / 125, rem = tau - mh * 125, nt = rem / 5, mt = mh * 5 + (rem - nt * 5);
    const int m0 = mt == 9 ? 2048 - 208 : mt * 208;
    if (nt < 10) k1_body<0>(x, nt < 5 ? wr : wi, D_ * D_ * 2, (nt < 5 ? nt : nt - 5) * 128, m0, nt, ws, lds0);
    else if (nt < 20) k1_body<1>(x, wqkv, D_ * 1920 * 2, (nt - 10) * 128, m0, nt, ws, lds0);
    else k1_body<2>(x, wqkv, D_ * 1920 * 2, (nt - 10) * 128, m0, nt, ws, lds0);
}

constexpr int K2_LDS = 2 * 36864;

DI float half_swap_max(float m) {
    auto r = __builtin_amdgcn_permlane32_swap(__float_as_uint(m), __float_as_uint(m), false, false);
    return fmaxf(__uint_as_float(r[0]), __uint_as_float(r[1]));
}
DI float max16(const f32x16& s) {
    float a = fmaxf(fmaxf(s[0], s[1]), s[2]), b = fmaxf(fmaxf(s[3], s[4]), s[5]);
    a = fmaxf(fmaxf(a, s[6]), s[7]); b = fmaxf(fmaxf(b, s[8]), s[9]);
    a = fmaxf(fmaxf(a, s[10]), s[11]); b = fmaxf(fmaxf(b, s[12]), s[13]);
    a = fmaxf(fmaxf(a, s[14]), s[15]);
    return fmaxf(a, b);
}
DI void exp_pack(const f32x16& s, bf16x8& p0, bf16x8& p1) {
    u32x4 a, b;
#pragma unroll
    for (int i = 0; i < 4; ++i) a[i] = pk(__builtin_amdgcn_exp2f(s[2 * i]), __builtin_amdgcn_exp2f(s[2 * i + 1]));
#pragma unroll
    for (int i = 0; i < 4; ++i) b[i] = pk(__builtin_amdgcn_exp2f(s[8 + 2 * i]), __builtin_amdgcn_exp2f(s[9 + 2 * i]));
    p0 = __builtin_bit_cast(bf16x8, a); p1 = __builtin_bit_cast(bf16x8, b);
}

DI void attn_role(int ab, char* ws, unsigned lds0) {
    const int tid = threadIdx.x, l = tid & 63, hh = l >> 5, r32 = l & 31;
    const int wv = __builtin_amdgcn_readfirstlane(tid >> 6);
    const int head = (ab & 7) * 4 + (ab >> 6), qt = ((ab >> 3) & 7) * 8 + wv;
    const rsrc_t rkv = mk_rsrc(ws + OFF_KV + head * KV_HEAD, KV_HEAD);
    const unsigned lane16 = l * 16;
    auto load_chunk = [&](int c, int slot) {
        const unsigned dst = lds0 + slot * KV_CHUNK + wv * 1024, src = c * KV_CHUNK + wv * 1024;
        dma16(dst, lane16, rkv, src);
        dma16(dst + 8192, lane16, rkv, src + 8192);
        if (wv < 6) dma16(dst + 16384, lane16, rkv, src + 16384);
    };
    load_chunk(0, 0);
    load_chunk(1, 1);
    const char* qp = ws + OFF_Q + head * Q_HEAD + qt * 1280;
    const bf16x8 q1 = *(const bf16x8*)(qp + l * 16);
    const u32x2 qr = *(const u32x2*)(qp + 1024 + r32 * 8);
    bf16x8 q2[4];
#pragma unroll
    for (int T = 0; T < 4; ++T) {
        const bool on = hh == (T >> 1);
        const unsigned a = on ? qr[0] : 0u, b = on ? qr[1] : 0u;
        const u32x4 v = (T & 1) ? u32x4{0u, 0u, a, b} : u32x4{a, b, 0u, 0u};
        q2[T] = __builtin_bit_cast(bf16x8, v);
    }
    const int dd = r32 <= 20 ? r32 : (r32 < 28 ? 20 : 19);
    const unsigned voff = 10240 + hh * 384 + dd * 16;
    const f32x16 zero16 = {0.f, 0.f, 0.f, 0.f, 0.f, 0.f, 0.f, 0.f, 0.f, 0.f, 0.f, 0.f, 0.f, 0.f, 0.f, 0.f};
    WAIT_VM(0);
    BAR();
    unsigned kb = lds0 + lane16, vb = lds0 + voff;
    bf16x8 k2f = lds_rd16(kb + 8192);
    f32x16 sc = MFMA32(lds_rd16(kb), q1, zero16);
    sc = MFMA32(k2f, q2[0], sc);
    const float m0 = half_swap_max(max16(sc));
    f32x16 negm;
#pragma unroll
    for (int i = 0; i < 16; ++i) { negm[i] = -m0; sc[i] -= m0; }
    f32x16 o = zero16;
    bf16x8 k1f = lds_rd16(kb + 1024);
    int slot = 0;
    for (int c = 0; c < 8; ++c) {
        const int nslot = slot == 2 ? 0 : slot + 1;
        const unsigned kbn = lds0 + nslot * KV_CHUNK + lane16;
#pragma unroll
        for (int t = 0; t < 8; ++t) {
            if (t == 3) {
                WAIT_VM(0);
                BAR();
                if (c + 2 < 8) load_chunk(c + 2, nslot == 2 ? 0 : nslot + 1);
            }
            if (t == 3) k2f = lds_rd16(kb + 8192 + 1024);
            if (t == 7) k2f = lds_rd16(kbn + 8192);
            f32x16 sn = MFMA32(k1f, q1, negm);
            sn = MFMA32(k2f, q2[(t + 1) & 3], sn);
            k1f = (t < 6) ? lds_rd16(kb + (t + 2) * 1024) : lds_rd16(kbn + (t - 6) * 1024);
            const bf16x8 v0 = lds_rd16(vb + t * 1536), v1 = lds_rd16(vb + t * 1536 + 768);
            bf16x8 p0, p1;
            exp_pack(sc, p0, p1);
            o = MFMA32(v0, p0, o);
            o = MFMA32(v1, p1, o);
            sc = sn;
        }
        slot = nslot; kb = kbn; vb = lds0 + nslot * KV_CHUNK + voff;
    }
    float lsum;
    {
        auto r = __builtin_amdgcn_permlane32_swap(__float_as_uint(o[8]), __float_as_uint(o[8]), false, false);
        lsum = __uint_as_float(r[1]);
    }
    float amax = fmaxf(fmaxf(fmaxf(fabsf(o[0]), fabsf(o[1])), fmaxf(fabsf(o[2]), fabsf(o[3]))), fmaxf(fmaxf(fabsf(o[4]), fabsf(o[5])), fmaxf(fabsf(o[6]), fabsf(o[7]))));
    const float osum = (o[0] + o[1] + o[2] + o[3]) + (o[4] + o[5] + o[6] + o[7]) + (hh ? 0.f : (o[8] + o[9] + o[10] + o[11]));
    amax = fmaxf(amax, hh ? 0.f : fmaxf(fmaxf(fabsf(o[8]), fabsf(o[9])), fmaxf(fabsf(o[10]), fabsf(o[11]))));
    const bool bad = !(lsum <= 1.0e18f) || !(amax <= 1.0e37f) || !(osum == osum);
    if (__builtin_expect(__any(bad), 0)) {
        const char* kv = ws + OFF_KV + head * KV_HEAD;
        float m = -INFINITY;
        o = zero16;
        for (int t = 0; t < 64; ++t) {
            const char* cb = kv + (t >> 3) * KV_CHUNK;
            const int T = t & 7;
            const bf16x8 ka = *(const bf16x8*)(cb + T * 1024 + lane16);
            const u32x2 kr = *(const u32x2*)(cb + 8192 + (T >> 1) * 512 + r32 * 16 + (T & 1) * 8);
            const bf16x8 kc = __builtin_bit_cast(bf16x8, u32x4{kr[0], kr[1], 0u, 0u});
            f32x16 s = MFMA32(ka, q1, zero16);
            s = MFMA32(kc, q2[0], s);
            const float mn = fmaxf(m, half_swap_max(max16(s)));
            const float al = __builtin_amdgcn_exp2f(m - mn);
            m = mn;
#pragma unroll
            for (int i = 0; i < 16; ++i) { o[i] *= al; s[i] -= mn; }
            bf16x8 p0, p1;
            exp_pack(s, p0, p1);
            o = MFMA32(*(const bf16x8*)(cb + voff + T * 1536), p0, o);
            o = MFMA32(*(const bf16x8*)(cb + voff + T * 1536 + 768), p1, o);
        }
        auto r = __builtin_amdgcn_permlane32_swap(__float_as_uint(o[8]), __float_as_uint(o[8]), false, false);
        lsum = __uint_as_float(r[1]);
    }
    const float rl = 1.f / lsum;
    const int q = qt * 32 + r32;
    char* ob = ws + OFF_O + (q >> 4) * 20480 + (q & 15) * 16;
#pragma unroll
    for (int gq = 0; gq < 3; ++gq) {
        if (gq == 2 && hh) break;
        const int k0 = head * 20 + 8 * gq + 4 * hh;
        const u32x2 v = {pk(o[4 * gq] * rl, o[4 * gq + 1] * rl), pk(o[4 * gq + 2] * rl, o[4 * gq + 3] * rl)};
        *(u32x2*)(ob + (k0 >> 5) * 1024 + ((k0 >> 3) & 3) * 256 + (k0 & 4) * 2) = v;
    }
}

template <int NE>
DI void dft_loop(unsigned lds0, int wv, int l, int e0, rsrc_t rc, rsrc_t rab, unsigned coff, unsigned csrc, unsigned absrc, char* ydst) {
    const int hh = l >> 5, r32 = l & 31, wm = wv & 3;
    const unsigned lane16 = l * 16;
    auto stage = [&](int ks) {
        const unsigned base = lds0 + (ks & 1) * 36864 + wv * 1024;
        dma16(base, coff, rc, csrc + ks * 128);
        dma16(base + 8192, coff, rc, csrc + ks * 128 + 64 * 4096);
        const unsigned as = absrc + ks * 20480 + wv * 1024;
        dma16(base + 16384, lane16, rab, as);
        dma16(base + 16384 + 8192, lane16, rab, as + 8192);
        if (NE == 2) dma16(base + 16384 + 16384 - 4096, lane16, rab, as + 16384 - 4096);
    };
    stage(0);
    const int row = wm * 32 + r32;
    const unsigned crd = lds0 + row * 128 + ((hh ^ ((row >> 1) & 7)) << 4);
    const unsigned ard = lds0 + 16384 + e0 * 1024 + lane16;
    f32x16 acc[NE];
#pragma unroll
    for (int e = 0; e < NE; ++e)
#pragma unroll
        for (int i = 0; i < 16; ++i) acc[e][i] = 0.f;
#pragma unroll 2
    for (int ks = 0; ks < 16; ++ks) {
        WAIT_VM(0);
        BAR();
        if (ks + 1 < 16) stage(ks + 1);
        const unsigned so = (ks & 1) * 36864;
#pragma unroll
        for (int kk = 0; kk < 4; ++kk) {
            const bf16x8 cf = lds_rd16((crd ^ (kk << 5)) + so);
#pragma unroll
            for (int e = 0; e < NE; ++e) acc[e] = MFMA32(lds_rd16(ard + so + (kk * 5 + e) * 1024), cf, acc[e]);
        }
    }
#pragma unroll
    for (int e = 0; e < NE; ++e)
#pragma unroll
        for (int s = 0; s < 2; ++s) {
            const u32x4 v = {pk(acc[e][8 * s], acc[e][8 * s + 1]), pk(acc[e][8 * s + 2], acc[e][8 * s + 3]),
                             pk(acc[e][8 * s + 4], acc[e][8 * s + 5]), pk(acc[e][8 * s + 6], acc[e][8 * s + 7])};
            *(u32x4*)(ydst + ((e0 + e) * 2 + s) * 1024 + lane16) = v;
        }
}

DI void dft_role(int gb, const __hip_bfloat16* x, const __hip_bfloat16* cr, const __hip_bfloat16* ci, const __hip_bfloat16* wo,
                 const __hip_bfloat16* gw, char* ws, unsigned lds0) {
    const int tid = threadIdx.x, l = tid & 63;
    const int wv = __builtin_amdgcn_readfirstlane(tid >> 6);
    const int xcd = gb & 7, slot = gb >> 3, kq = xcd & 3, mt = (xcd >> 2) * 8 + (slot >> 2), ntile = slot & 3;
    const rsrc_t rc = mk_rsrc(kq < 2 ? cr : ci, S_ * S_ * 2), rab = mk_rsrc(ws + OFF_AB, 4 * 64 * 20480);
    const unsigned coff = (unsigned)((8 * wv + (l >> 3)) * 4096 + (((l & 7) ^ ((4 * wv + (l >> 4)) & 7)) << 4));
    const unsigned csrc = (unsigned)(mt * 128 * 4096 + (kq & 1) * 2048);
    const unsigned absrc = (unsigned)((ntile * 64 + kq * 16) * 20480);
    char* ydst = ws + OFF_Y + (unsigned)(((kq * 64 + mt * 4 + (wv & 3)) * 40 + ntile * 10) * 1024);
    if (wv < 4) dft_loop<3>(lds0, wv, l, 0, rc, rab, coff, csrc, absrc, ydst);
    else dft_loop<2>(lds0, wv, l, 3, rc, rab, coff, csrc, absrc, ydst);
    {
        const int row = gb * 8 + wv;
        const unsigned* xr = (const unsigned*)(x + row * D_);
        const unsigned* gp = (const unsigned*)gw;
        float s = 0.f;
#pragma unroll
        for (int i = 0; i < 5; ++i) {
            const unsigned a = xr[l + 64 * i], b = gp[l + 64 * i];
            s += bf_lo(a) * bf_lo(b) + bf_hi(a) * bf_hi(b);
        }
#pragma unroll
        for (int off = 32; off > 0; off >>= 1) s += __shfl_xor(s, off);
        if (l == 0) ((float*)(ws + OFF_G))[row] = 1.f / (1.f + __expf(-s));
    }
    const int fr = gb * 8 + wv;
    if (fr < 800) {
        const int e16 = fr / 20, ks = fr - e16 * 20;
        const unsigned short* wp = (const unsigned short*)wo + (ks * 32 + 8 * (l >> 4)) * D_ + e16 * 16 + (l & 15);
        u32x4 v;
#pragma unroll
        for (int j = 0; j < 4; ++j) v[j] = (unsigned)wp[(2 * j) * D_] | ((unsigned)wp[(2 * j + 1) * D_] << 16);
        *(u32x4*)(ws + OFF_WOT + fr * 1024 + l * 16) = v;
    }
}

__global__ __launch_bounds__(512, 4) void k2_mix(const __hip_bfloat16* x, const __hip_bfloat16* cr, const __hip_bfloat16* ci,
                                                 const __hip_bfloat16* wo, const __hip_bfloat16* gw, char* ws) {
    extern __shared__ __attribute__((aligned(16))) char smem[];
    const unsigned lds0 = (unsigned)(uintptr_t)(LDSP char*)smem;
    const int b = blockIdx.x;
    if (b < 256) attn_role(b, ws, lds0);
    else dft_role(b - 256, x, cr, ci, wo, gw, ws, lds0);
}

constexpr int K3_LDS = 4 * 64 * 336;

__global__ __launch_bounds__(512, 2) void k3_out(const char* ws, __hip_bfloat16* out) {
    extern __shared__ __attribute__((aligned(16))) char smem[];
    const int tid = threadIdx.x, l = tid & 63;
    const int wv = __builtin_amdgcn_readfirstlane(tid >> 6);
    const int b = blockIdx.x, mt = (b & 7) * 4 + (b >> 6), nt = (b >> 3) & 7;
    const int kq = wv & 3, th = wv >> 2;
    bf16x8 wf[5][5], of[5][2];
    const char* wp = ws + OFF_WOT + (unsigned)((nt * 5 * 20 + kq * 5) * 1024 + l * 16);
    const char* op = ws + OFF_O + (unsigned)(((mt * 4 + th * 2) * 20 + kq * 5) * 1024 + l * 16);
#pragma unroll
    for (int k = 0; k < 5; ++k) {
#pragma unroll
        for (int t = 0; t < 2; ++t) of[k][t] = *(const bf16x8*)(op + (t * 20 + k) * 1024);
#pragma unroll
        for (int e = 0; e < 5; ++e) wf[k][e] = *(const bf16x8*)(wp + (e * 20 + k) * 1024);
    }
    u32x4 yv[2][4]; float gv[2];
#pragma unroll
    for (int it = 0; it < 2; ++it) {
        const int item = it == 0 ? wv : (wv & 1) + 8;
        const int t32 = item / 5, e16 = item - t32 * 5;
#pragma unroll
        for (int p = 0; p < 4; ++p)
            yv[it][p] = *(const u32x4*)(ws + OFF_Y + (unsigned)(((p * 64 + mt * 2 + t32) * 40 + nt * 5 + e16) * 1024 + l * 16));
        gv[it] = ((const float*)(ws + OFF_G))[mt * 64 + t32 * 32 + (l & 31)];
    }
    f32x4 acc[5][2];
#pragma unroll
    for (int e = 0; e < 5; ++e) { acc[e][0] = f32x4{0.f, 0.f, 0.f, 0.f}; acc[e][1] = f32x4{0.f, 0.f, 0.f, 0.f}; }
#pragma unroll
    for (int k = 0; k < 5; ++k)
#pragma unroll
        for (int e = 0; e < 5; ++e)
#pragma unroll
            for (int t = 0; t < 2; ++t) acc[e][t] = MFMA16(wf[k][e], of[k][t], acc[e][t]);
#pragma unroll
    for (int e = 0; e < 5; ++e)
#pragma unroll
        for (int t = 0; t < 2; ++t)
            *(f32x4*)(smem + (kq * 64 + th * 32 + t * 16 + (l & 15)) * 336 + (e * 16 + 4 * (l >> 4)) * 4) = acc[e][t];
    __syncthreads();
#pragma unroll
    for (int it = 0; it < 2; ++it) {
        if (it == 1 && wv >= 2) break;
        const int item = it == 0 ? wv : wv + 8;
        const int t32 = item / 5, e16 = item - t32 * 5;
        const int tl = t32 * 32 + (l & 31), el = e16 * 16 + (l >> 5) * 8;
        f32x4 s0 = {0.f, 0.f, 0.f, 0.f}, s1 = {0.f, 0.f, 0.f, 0.f};
#pragma unroll
        for (int p = 0; p < 4; ++p) {
            s0 += *(const f32x4*)(smem + (p * 64 + tl) * 336 + el * 4);
            s1 += *(const f32x4*)(smem + (p * 64 + tl) * 336 + el * 4 + 16);
        }
        float y[8];
#pragma unroll
        for (int j = 0; j < 4; ++j) {
            y[2 * j] = (bf_lo(yv[it][0][j]) + bf_lo(yv[it][1][j])) + (bf_lo(yv[it][2][j]) + bf_lo(yv[it][3][j]));
            y[2 * j + 1] = (bf_hi(yv[it][0][j]) + bf_hi(yv[it][1][j])) + (bf_hi(yv[it][2][j]) + bf_hi(yv[it][3][j]));
        }
        const float g = gv[it], g1 = 1.f - g;
        const u32x4 v = {pk(g * y[0] + g1 * s0[0], g * y[1] + g1 * s0[1]), pk(g * y[2] + g1 * s0[2], g * y[3] + g1 * s0[3]),
                         pk(g * y[4] + g1 * s1[0], g * y[5] + g1 * s1[1]), pk(g * y[6] + g1 * s1[2], g * y[7] + g1 * s1[3])};
        *(u32x4*)((char*)out + (unsigned)((mt * 64 + tl) * (D_ * 2) + (nt * 80 + el) * 2)) = v;
    }
}

extern "C" void kernel_launch(void* const* d_in, const int* in_sizes, int n_in, void* d_out, int out_size, void* d_ws,
                              size_t ws_size, hipStream_t stream) {
    const __hip_bfloat16* x = (const __hip_bfloat16*)d_in[0];
    const __hip_bfloat16* cr = (const __hip_bfloat16*)d_in[1];
    const __hip_bfloat16* ci = (const __hip_bfloat16*)d_in[2];
    const __hip_bfloat16* wr = (const __hip_bfloat16*)d_in[3];
    const __hip_bfloat16* wi = (const __hip_bfloat16*)d_in[4];
    const __hip_bfloat16* wqkv = (const __hip_bfloat16*)d_in[6];
    const __hip_bfloat16* wo = (const __hip_bfloat16*)d_in[7];
    const __hip_bfloat16* gw = (const __hip_bfloat16*)d_in[8];
    char* ws = (char*)d_ws;
    (void)hipFuncSetAttribute((const void*)k1_proj, hipFuncAttributeMaxDynamicSharedMemorySize, K1_LDS);
    (void)hipFuncSetAttribute((const void*)k2_mix, hipFuncAttributeMaxDynamicSharedMemorySize, K2_LDS);
    (void)hipFuncSetAttribute((const void*)k3_out, hipFuncAttributeMaxDynamicSharedMemorySize, K3_LDS);
    k1_proj<<<250, 512, K1_LDS, stream>>>(x, wr, wi, wqkv, ws);
    k2_mix<<<512, 512, K2_LDS, stream>>>(x, cr, ci, wo, gw, ws);
    k3_out<<<256, 512, K3_LDS, stream>>>(ws, (__hip_bfloat16*)d_out);
}
```

```cpp
#include <hip/hip_runtime.h>
#include <hip/hip_bf16.h>
#include <stdint.h>

#define FUSED 1

typedef short bf16x8 __attribute__((ext_vector_type(8)));
typedef short s16x4 __attribute__((ext_vector_type(4)));
typedef float f32x16 __attribute__((ext_vector_type(16)));
typedef float f32x4 __attribute__((ext_vector_type(4)));
typedef float f32x2 __attribute__((ext_vector_type(2)));
typedef unsigned u32x4 __attribute__((ext_vector_type(4)));
typedef unsigned u32x2 __attribute__((ext_vector_type(2)));
typedef __bf16 bf16x2_t __attribute__((ext_vector_type(2)));
typedef unsigned long long u64;
typedef __attribute__((address_space(1))) u64 gu64;
typedef __hip_bfloat16 bf16;
#define LDSP __attribute__((address_space(3)))
#define DI __device__ __forceinline__
typedef __amdgpu_buffer_rsrc_t rsrc_t;

constexpr int S_ = 2048, D_ = 640, H_ = 32;
constexpr unsigned OFF_EPOCH = 0, OFF_FA1 = 4096, OFF_FG1 = 8192, OFF_FG2 = 12288, OFF_FA2 = 16384;
constexpr unsigned OFF_Q1 = 32768;
constexpr unsigned OFF_Q2 = OFF_Q1 + H_ * 65536;
constexpr unsigned OFF_K2 = OFF_Q2 + 16 * 32768;
constexpr unsigned KV_CHUNK = 20480, KV_HEAD = 8 * KV_CHUNK;
constexpr unsigned OFF_KV = OFF_K2 + 16 * 32768;
constexpr unsigned OFF_AB = OFF_KV + H_ * KV_HEAD;
constexpr unsigned OFF_Y = OFF_AB + 4 * 64 * 20480;
constexpr unsigned OFF_O = OFF_Y + 4 * S_ * D_ * 2;
constexpr unsigned OFF_G = OFF_O + S_ * D_ * 2;
constexpr unsigned WS_BYTES = OFF_G + S_ * 4;
constexpr int LDS_BYTES = 81920;
constexpr unsigned SPIN_CAP = 1u << 16;

#define STAMP(ws, i) do { } while (0)
#define TDECL
#define TREC(i) do { } while (0)
#define TDUMP(ws, n) do { } while (0)
#define MFMA32(a, b, c) __builtin_amdgcn_mfma_f32_32x32x16_bf16((a), (b), (c), 0, 0, 0)
#define MFMA16(a, b, c) __builtin_amdgcn_mfma_f32_16x16x32_bf16((a), (b), (c), 0, 0, 0)
#define WAIT_VM(n) asm volatile("s_waitcnt vmcnt(" #n ")" ::: "memory")
#define WAIT_VM_LGKM0(n) asm volatile("s_waitcnt vmcnt(" #n ") lgkmcnt(0)" ::: "memory")
#define BAR() asm volatile("s_barrier" ::: "memory")
#define EX2(v) __builtin_amdgcn_exp2f(v)
#define PIN(v) asm volatile("" : "+v"(v))
#define SB() __builtin_amdgcn_sched_barrier(0)
#define RLX_AGENT __ATOMIC_RELAXED, __HIP_MEMORY_SCOPE_AGENT

DI unsigned pk(float a, float b) { f32x2 v = {a, b}; return __builtin_bit_cast(unsigned, __builtin_convertvector(v, bf16x2_t)); }
DI float bf_lo(unsigned u) { return __uint_as_float(u << 16); }
DI float bf_hi(unsigned u) { return __uint_as_float(u & 0xffff0000u); }
DI rsrc_t mk_rsrc(const void* p, unsigned bytes) { return __builtin_amdgcn_make_buffer_rsrc((void*)p, (short)0, (int)bytes, 0x00020000); }
DI void dma16(unsigned lds_addr, unsigned voff, rsrc_t rs, unsigned soff) {
    asm volatile("s_mov_b32 m0, %0\n\ts_nop 0\n\tbuffer_load_dwordx4 %1, %2, %3 offen lds" ::"s"(lds_addr), "v"(voff), "s"(rs), "s"(soff) : "memory");
}
DI void dma16c(unsigned lds_addr, unsigned voff, rsrc_t rs, unsigned soff) {
    asm volatile("s_mov_b32 m0, %0\n\ts_nop 0\n\tbuffer_load_dwordx4 %1, %2, %3 offen sc1 lds" ::"s"(lds_addr), "v"(voff), "s"(rs), "s"(soff) : "memory");
}
DI bf16x8 lds_rd16(unsigned a) { return *(const LDSP bf16x8*)(a); }
DI s16x4 lds_tr(unsigned a) { return __builtin_amdgcn_ds_read_tr16_b64_v4i16((LDSP s16x4*)(a)); }
DI bf16x8 lds_tr2(unsigned a0, unsigned a1) { return __builtin_shufflevector(lds_tr(a0), lds_tr(a1), 0, 1, 2, 3, 4, 5, 6, 7); }
DI void st16(rsrc_t rs, unsigned off, u32x4 v) { __builtin_amdgcn_raw_buffer_store_b128(v, rs, off, 0, 16); }
DI void st8(rsrc_t rs, unsigned off, u32x2 v) { __builtin_amdgcn_raw_buffer_store_b64(v, rs, off, 0, 16); }
DI void st4(rsrc_t rs, unsigned off, unsigned v) { __builtin_amdgcn_raw_buffer_store_b32(v, rs, off, 0, 16); }
DI u32x4 ld16(rsrc_t rs, unsigned off) { return __builtin_amdgcn_raw_buffer_load_b128(rs, off, 0, 16); }
DI int swap23(int i) { return (i & ~12) | ((i & 4) << 1) | ((i & 8) >> 1); }
DI u32x4 pack8(const f32x4& a, const f32x4& b) { return u32x4{pk(a[0], a[1]), pk(a[2], a[3]), pk(b[0], b[1]), pk(b[2], b[3])}; }

#define POLL_SLEEP 4
#define G_DELAY 6
#define A_DELAY 0
#define G_PRIO 2
#define DFT_PRIO 0
#define A_PRIO 3
#define ATT_PRIO 0
#define WARM 1
#define DFT_HI 6
#define SWAP_ROLES 0
DI void publish(char* ws, unsigned flag_off, u64 tag) {
    WAIT_VM(0);
    BAR();
    if (threadIdx.x == 0) __hip_atomic_store((gu64*)(ws + flag_off), tag, RLX_AGENT);
}
DI void publish_wave(char* ws, unsigned flag_off, u64 tag) {
    WAIT_VM(0);
    if ((threadIdx.x & 63) == 0) __hip_atomic_store((gu64*)(ws + flag_off), tag, RLX_AGENT);
}
DI void await_wave(char* ws, unsigned off, u64 tag) {
    _Pragma("unroll 1") for (unsigned spins = 0; spins < SPIN_CAP; ++spins) {
        if (__all(__hip_atomic_load((gu64*)(ws + off), RLX_AGENT) == tag)) break;
        __builtin_amdgcn_s_sleep(POLL_SLEEP);
    }
    asm volatile("" ::: "memory");
}
DI void await2(char* ws, unsigned off0, unsigned off1, u64 tag, int wv) {
    if (wv == 0) {
        for (unsigned spins = 0; spins < (1u << 22); ++spins) {
            const u64 a = __hip_atomic_load((gu64*)(ws + off0), RLX_AGENT), b = __hip_atomic_load((gu64*)(ws + off1), RLX_AGENT);
            if (__all(a == tag && b == tag)) break;
            __builtin_amdgcn_s_sleep(POLL_SLEEP);
        }
    }
    asm volatile("s_waitcnt lgkmcnt(0)\n\ts_barrier" ::: "memory");
}

template <int KIND>
DI void qkv_body(const bf16* x, const bf16* wqkv, int m0, int nt, char* ws, unsigned lds0, int wv, int bid) {
    const int l = threadIdx.x & 63, wn = wv & 3, wm = wv >> 2, i16 = l & 15, g = l >> 4;
    const rsrc_t rx = mk_rsrc(x, S_ * D_ * 2), rw = mk_rsrc(wqkv, D_ * 1920 * 2);
    unsigned xoff, woff;
    {
        const int rho = 8 * wv + (l >> 3), i = rho & 15;
        const int s = KIND == 2 ? (rho & ~31) + 8 * (i >> 2) + 4 * ((rho >> 4) & 1) + (i & 3) : rho;
        xoff = (unsigned)((m0 + s) * (D_ * 2) + (((l & 7) ^ (l >> 3)) << 4));
        const int k = 4 * wv + (l >> 4), f = (k & 3) | (((k >> 3) & 1) << 2);
        woff = (unsigned)(k * 3840 + nt * 256 + (((i16 >> 1) ^ f) << 5) + ((i16 & 1) << 4));
    }
    auto piece = [&](int u, int i) {
        const unsigned dst = lds0 + (u % 5) * 16384 + i * 8192 + wv * 1024;
        if (u & 1) dma16(dst, woff, rw, ((u >> 1) * 64 + i * 32) * 3840);
        else dma16(dst, xoff, rx, (u >> 1) * 128 + i * 64 * (D_ * 2));
    };
    TDECL; TREC(0);
#pragma unroll
    for (int u = 0; u < 5; ++u) { piece(u, 0); piece(u, 1); if (u == 1) BAR(); }
    const unsigned xrd = lds0 + (wm * 64 + i16) * 128 + ((g ^ (i16 & 7)) << 4);
    const unsigned wrd = lds0 + (8 * g + (i16 >> 2)) * 256 + (((2 * wn) ^ ((i16 >> 2) | ((g & 1) << 2))) << 5) + 8 * (l & 3);
    f32x4 acc[4][2];
#pragma unroll
    for (int m = 0; m < 4; ++m) { acc[m][0] = f32x4{0.f, 0.f, 0.f, 0.f}; acc[m][1] = f32x4{0.f, 0.f, 0.f, 0.f}; }
    bf16x8 xf[2][4], wf[2][2];
    auto rd1 = [&](int kt, int kk, int i) {
        const unsigned xs = ((2 * kt) % 5) * 16384, wsl = ((2 * kt + 1) % 5) * 16384;
        if (i < 2) { const unsigned a = (wrd ^ (i << 5)) + wsl + kk * 8192; wf[kk][i] = lds_tr2(a, a + 1024); }
        else if (i < 6) xf[kk][i - 2] = lds_rd16((xrd ^ (kk << 6)) + xs + (i - 2) * 2048);
    };
    auto mm = [&](int kk, int m) {
#pragma unroll
        for (int j = 0; j < 2; ++j) acc[m][j] = (KIND == 1) ? MFMA16(wf[kk][j], xf[kk][m], acc[m][j]) : MFMA16(xf[kk][m], wf[kk][j], acc[m][j]);
    };
#pragma unroll
    for (int kt = 0; kt < 10; ++kt) {
        TREC(3 * kt + 1);
        if (kt == 0) WAIT_VM_LGKM0(6); else if (kt < 9) WAIT_VM_LGKM0(2); else WAIT_VM_LGKM0(0);
        TREC(3 * kt + 2);
        BAR();
        TREC(3 * kt + 3);
        SB();
#pragma unroll
        for (int m = 0; m < 4; ++m) {
            if (kt > 0) mm(1, m);
            SB();
            rd1(kt, 0, 2 * m); rd1(kt, 0, 2 * m + 1);
            if (kt > 0 && 2 * kt + 3 + (m >> 1) < 20) piece(2 * kt + 3 + (m >> 1), m & 1);
            SB();
        }
#pragma unroll
        for (int m = 0; m < 4; ++m) {
            mm(0, m);
            SB();
            rd1(kt, 1, 2 * m); rd1(kt, 1, 2 * m + 1);
            SB();
        }
    }
#pragma unroll
    for (int m = 0; m < 4; ++m) mm(1, m);
    TREC(31); TDUMP(ws, 32);
    STAMP(ws, 1);
    const rsrc_t rws = mk_rsrc(ws, WS_BYTES);
    if (KIND == 2) {
#pragma unroll
        for (int j = 0; j < 2; ++j) {
            const int col = (nt - 10) * 128 + wn * 32 + 16 * j + i16;
            const int h = col / 20, d = col - h * 20;
            const int sg = (h & 1) ? (d >= 4 ? d - 4 : d + 16) : d;
            const unsigned base = OFF_KV + (unsigned)(h * KV_HEAD + 8192);
            const unsigned pos = (unsigned)((sg < 16 ? swap23(sg) : sg) * 16);
#pragma unroll
            for (int u = 0; u < 2; ++u) {
                const int s = m0 + wm * 64 + 32 * u + 8 * g;
                const unsigned dst = base + (s >> 8) * KV_CHUNK + ((s & 255) >> 3) * 384;
                st16(rws, dst + pos, pack8(acc[2 * u][j], acc[2 * u + 1][j]));
                if (d == 19) {
                    st16(rws, dst + 320, u32x4{0x3F803F80u, 0x3F803F80u, 0x3F803F80u, 0x3F803F80u});
                    st16(rws, dst + 336, u32x4{0u, 0u, 0u, 0u});
                }
            }
        }
    } else {
        const bool isq = nt < 5;
        const float sc = isq ? 0.22360679774997896f * 1.4426950408889634f : 1.f;
        const int pos = isq ? (l & 31) : swap23(l & 31);
#pragma unroll
        for (int j = 0; j < 2; ++j) {
            const int c = (isq ? nt : nt - 5) * 128 + wn * 32 + 16 * j + 8 * (l >> 5);
            const int pair = c / 40, oc = (c - pair * 40) >> 3;
            const int h = 2 * pair + (oc > 2), half = oc > 2 ? oc - 3 : oc;
#pragma unroll
            for (int u = 0; u < 2; ++u) {
                const f32x4 a = acc[2 * u][j] * sc, b = acc[2 * u + 1][j] * sc;
                const auto r0 = __builtin_amdgcn_permlane16_swap(pk(a[0], a[1]), pk(b[0], b[1]), false, false);
                const auto r1 = __builtin_amdgcn_permlane16_swap(pk(a[2], a[3]), pk(b[2], b[3]), false, false);
                const int tile = (m0 + wm * 64 + 32 * u) >> 5;
                unsigned off;
                if (oc == 2) off = (isq ? OFF_Q2 : OFF_K2) + pair * 32768 + (tile * 32 + pos) * 16;
                else if (isq) off = OFF_Q1 + h * 65536 + tile * 1024 + (half * 32 + pos) * 16;
                else off = OFF_KV + h * KV_HEAD + (tile >> 3) * KV_CHUNK + (tile & 7) * 1024 + (half * 32 + pos) * 16;
                st16(rws, off, u32x4{r0[0], r1[0], r0[1], r1[1]});
            }
        }
    }
}

template <int WN>
DI void ab_body(const bf16* x, const bf16* w, bool isb, int m0, int n0, char* ws, unsigned lds0, int wv, int bid) {
    constexpr int NJ = WN == 0 ? 3 : 2, STG = 26624;
    const int l = threadIdx.x & 63, wm = wv & 3, i16 = l & 15, g = l >> 4;
    const rsrc_t rx = mk_rsrc(x, S_ * D_ * 2), rw = mk_rsrc(w, D_ * D_ * 2);
    unsigned xoff, woff;
    {
        const int rho = 8 * wv + (l >> 3), i = rho & 15;
        const int s = (rho & ~31) + 8 * (i >> 2) + 4 * ((rho >> 4) & 1) + (i & 3);
        xoff = (unsigned)((m0 + s) * (D_ * 2) + (((l & 7) ^ (l >> 3)) << 4));
        woff = (unsigned)((n0 + rho) * (D_ * 2) + (((l & 7) ^ (l >> 3)) << 4));
    }
    const bool extra = WN == 1 && wv < 6;
    auto piece = [&](int kt, int i) {
        const unsigned dst = lds0 + (kt % 3) * STG + wv * 1024;
        if (i < 2) dma16(dst + i * 8192, xoff, rx, kt * 128 + i * 64 * (D_ * 2));
        else if (i == 2) dma16(dst + 16384, woff, rw, kt * 128);
        else if (WN == 1) { if (extra) dma16(dst + 16384 + 4096, woff, rw, kt * 128 + 32 * (D_ * 2)); }
    };
#pragma unroll
    for (int st = 0; st < 2; ++st)
#pragma unroll
        for (int i = 0; i < 4; ++i) piece(st, i);
    const unsigned xrd = lds0 + (wm * 32 + i16) * 128 + ((g ^ (i16 & 7)) << 4);
    const unsigned wrd = lds0 + 16384 + (WN * 48 + i16) * 128 + ((g ^ (i16 & 7)) << 4);
    f32x4 acc[2][NJ];
#pragma unroll
    for (int j = 0; j < NJ; ++j) { acc[0][j] = f32x4{0.f, 0.f, 0.f, 0.f}; acc[1][j] = f32x4{0.f, 0.f, 0.f, 0.f}; }
    bf16x8 xf[2][2], wf[2][NJ];
    auto rd1 = [&](int kt, int kk, int i) {
        const unsigned so = (kt % 3) * STG;
        if (i < 2) xf[kk][i] = lds_rd16((xrd ^ (kk << 6)) + so + i * 2048);
        else if (i - 2 < NJ) wf[kk][i - 2] = lds_rd16((wrd ^ (kk << 6)) + so + (i - 2) * 2048);
    };
    auto mm = [&](int kk, int j) {
#pragma unroll
        for (int m = 0; m < 2; ++m) acc[m][j] = MFMA16(xf[kk][m], wf[kk][j], acc[m][j]);
    };
#pragma unroll
    for (int kt = 0; kt < 10; ++kt) {
        if (kt < 9) { if (extra) WAIT_VM_LGKM0(4); else WAIT_VM_LGKM0(3); } else WAIT_VM_LGKM0(0);
        BAR();
        SB();
#pragma unroll
        for (int j = 0; j < NJ; ++j) {
            if (kt > 0) mm(1, j);
            SB();
            rd1(kt, 0, 2 * j); rd1(kt, 0, 2 * j + 1);
            if (kt + 2 < 10 && j < 2) piece(kt + 2, j);
            SB();
        }
#pragma unroll
        for (int j = 0; j < NJ; ++j) {
            mm(0, j);
            SB();
            rd1(kt, 1, 2 * j); rd1(kt, 1, 2 * j + 1);
            if (kt + 2 < 10 && j < 2) piece(kt + 2, 2 + j);
            SB();
        }
    }
#pragma unroll
    for (int j = 0; j < NJ; ++j) mm(1, j);
    STAMP(ws, 1);
    const rsrc_t rws = mk_rsrc(ws, WS_BYTES);
    const float sg = isb ? -1.f : 1.f;
    const int kg = (isb ? 2048 : 0) + m0 + 32 * wm + 8 * g;
#pragma unroll
    for (int j = 0; j < NJ; ++j) {
        const int e = n0 + WN * 48 + 16 * j + i16;
        const int ntile = e / 160, er = e - ntile * 160;
        st16(rws, OFF_AB + (unsigned)(ntile * 64 * 20480 + (er >> 5) * 1024 + swap23(er & 31) * 16 + (kg >> 4) * 5120 + ((kg >> 3) & 1) * 512),
             pack8(acc[0][j] * sg, acc[1][j] * sg));
    }
}

constexpr int AT_SLOT = 24576;

DI float half_swap_max(float m) {
    auto r = __builtin_amdgcn_permlane32_swap(__float_as_uint(m), __float_as_uint(m), false, false);
    return fmaxf(__uint_as_float(r[0]), __uint_as_float(r[1]));
}
DI float max16(const f32x16& s) {
    float a = fmaxf(fmaxf(s[0], s[1]), s[2]), b = fmaxf(fmaxf(s[3], s[4]), s[5]);
    a = fmaxf(fmaxf(a, s[6]), s[7]); b = fmaxf(fmaxf(b, s[8]), s[9]);
    a = fmaxf(fmaxf(a, s[10]), s[11]); b = fmaxf(fmaxf(b, s[12]), s[13]);
    a = fmaxf(fmaxf(a, s[14]), s[15]);
    return fmaxf(a, b);
}

DI void attn_phase(int head, int qb, char* ws, unsigned lds0, int wv, int bid) {
    const int l = threadIdx.x & 63, hh = l >> 5, r32 = l & 31;
    const int qt = qb * 8 + wv, pair = head >> 1, odd = head & 1;
    const rsrc_t rws = mk_rsrc(ws, WS_BYTES);
    const unsigned lane16 = l * 16;
    const unsigned kvsrc = OFF_KV + head * KV_HEAD + wv * 1024;
    const unsigned src3 = wv < 4 ? kvsrc + 16384 : OFF_K2 + pair * 32768 + (wv - 4) * 1024, step3 = wv < 4 ? KV_CHUNK : 4096;
    auto load_chunk = [&](int c, int slot) {
        const unsigned dst = lds0 + slot * AT_SLOT + wv * 1024;
        dma16c(dst, lane16, rws, kvsrc + c * KV_CHUNK);
        dma16c(dst + 8192, lane16, rws, kvsrc + c * KV_CHUNK + 8192);
        dma16c(dst + 16384, lane16, rws, src3 + c * step3);
    };
    load_chunk(0, 0);
    const bf16x8 q1 = __builtin_bit_cast(bf16x8, ld16(rws, OFF_Q1 + head * 65536 + qt * 1024 + lane16));
    const u32x4 qr = ld16(rws, OFF_Q2 + pair * 32768 + (qt * 32 + r32) * 16);
    load_chunk(1, 1);
    bf16x8 q2[2];
#pragma unroll
    for (int P = 0; P < 2; ++P) {
        const bool lo = hh == P && !odd, hi = hh == P && odd;
        q2[P] = __builtin_bit_cast(bf16x8, u32x4{lo ? qr[0] : 0u, lo ? qr[1] : 0u, hi ? qr[2] : 0u, hi ? qr[3] : 0u});
    }
    const unsigned voff = 8192 + hh * 384 + (r32 <= 20 ? r32 : 21) * 16;
    const f32x16 zero16 = {0.f, 0.f, 0.f, 0.f, 0.f, 0.f, 0.f, 0.f, 0.f, 0.f, 0.f, 0.f, 0.f, 0.f, 0.f, 0.f};
    WAIT_VM(3);
    BAR();
    STAMP(ws, 4);
    unsigned kb = lds0 + lane16, vb = lds0 + voff;
    bf16x8 k2f = lds_rd16(kb + 20480);
    f32x16 sc = MFMA32(lds_rd16(kb), q1, zero16);
    sc = MFMA32(k2f, q2[0], sc);
    const float m0 = half_swap_max(max16(sc));
    f32x16 negm;
#pragma unroll
    for (int i = 0; i < 16; ++i) { negm[i] = -m0; sc[i] -= m0; }
    f32x16 o = zero16;
    bf16x8 k1f = lds_rd16(kb + 1024);
    bf16x8 v0f = lds_rd16(vb), v1f = lds_rd16(vb);
    u32x4 p0, p1 = {0u, 0u, 0u, 0u};
    float e12 = 0.f, e13 = 0.f, e14 = 0.f, e15 = 0.f;
    int slot = 0;
    for (int c = 0; c < 8; ++c) {
        const int nslot = slot == 2 ? 0 : slot + 1;
        const unsigned kbn = lds0 + nslot * AT_SLOT + lane16, vbn = lds0 + nslot * AT_SLOT + voff;
#pragma unroll
        for (int t = 0; t < 8; ++t) {
            if (t == 2) {
                WAIT_VM(0);
                BAR();
                if (c + 2 < 8) load_chunk(c + 2, nslot == 2 ? 0 : nslot + 1);
            }
            float a0, a1, a2, a3, b0, b1, b2, b3;
            SB();
            f32x16 sn = MFMA32(k1f, q1, negm);
            k1f = (t < 6) ? lds_rd16(kb + (t + 2) * 1024) : lds_rd16(kbn + (t - 6) * 1024);
            p1[2] = pk(e12, e13); p1[3] = pk(e14, e15); PIN(p1);
            a0 = EX2(sc[0]); a1 = EX2(sc[1]); a2 = EX2(sc[2]); a3 = EX2(sc[3]); PIN(a0); PIN(a1); PIN(a2); PIN(a3);
            SB();
            sn = MFMA32(k2f, q2[(t + 1) & 1], sn);
            if ((t & 1) == 0) k2f = (t < 6) ? lds_rd16(kb + 20480 + (t + 2) * 512) : lds_rd16(kbn + 20480);
            p0[0] = pk(a0, a1); p0[1] = pk(a2, a3);
            b0 = EX2(sc[4]); b1 = EX2(sc[5]); b2 = EX2(sc[6]); b3 = EX2(sc[7]); PIN(b0); PIN(b1); PIN(b2); PIN(b3);
            SB();
            o = MFMA32(v1f, __builtin_bit_cast(bf16x8, p1), o);
            v1f = lds_rd16(vb + t * 1536 + 768);
            p0[2] = pk(b0, b1); p0[3] = pk(b2, b3); PIN(p0);
            a0 = EX2(sc[8]); a1 = EX2(sc[9]); a2 = EX2(sc[10]); a3 = EX2(sc[11]); PIN(a0); PIN(a1); PIN(a2); PIN(a3);
            SB();
            o = MFMA32(v0f, __builtin_bit_cast(bf16x8, p0), o);
            v0f = (t < 7) ? lds_rd16(vb + (t + 1) * 1536) : lds_rd16(vbn);
            p1[0] = pk(a0, a1); p1[1] = pk(a2, a3);
            e12 = EX2(sc[12]); e13 = EX2(sc[13]); e14 = EX2(sc[14]); e15 = EX2(sc[15]); PIN(e12); PIN(e13); PIN(e14); PIN(e15);
            SB();
            sc = sn;
        }
        slot = nslot; kb = kbn; vb = vbn;
    }
    p1[2] = pk(e12, e13); p1[3] = pk(e14, e15);
    o = MFMA32(v1f, __builtin_bit_cast(bf16x8, p1), o);
    STAMP(ws, 5);
    float lsum;
    {
        auto r = __builtin_amdgcn_permlane32_swap(__float_as_uint(o[8]), __float_as_uint(o[8]), false, false);
        lsum = __uint_as_float(r[1]);
    }
    float asum = 0.f;
#pragma unroll
    for (int i = 0; i < 12; ++i) asum += fabsf(o[i]);
    const bool bad = !(lsum <= 1.0e18f) || !(asum <= 1.0e37f);
    if (__builtin_expect(__any(bad), 0)) {
        float m = -INFINITY;
        o = zero16;
        for (int t = 0; t < 64; ++t) {
            const unsigned cb = OFF_KV + head * KV_HEAD + (t >> 3) * KV_CHUNK;
            const int T = t & 7;
            f32x16 s = MFMA32(__builtin_bit_cast(bf16x8, ld16(rws, cb + T * 1024 + lane16)), q1, zero16);
            s = MFMA32(__builtin_bit_cast(bf16x8, ld16(rws, OFF_K2 + pair * 32768 + (t * 32 + r32) * 16)), q2[0], s);
            const float mn = fmaxf(m, half_swap_max(max16(s)));
            const float al = EX2(m - mn);
            m = mn;
#pragma unroll
            for (int i = 0; i < 16; ++i) { o[i] *= al; s[i] = EX2(s[i] - mn); }
            const u32x4 pa = {pk(s[0], s[1]), pk(s[2], s[3]), pk(s[4], s[5]), pk(s[6], s[7])};
            const u32x4 pb = {pk(s[8], s[9]), pk(s[10], s[11]), pk(s[12], s[13]), pk(s[14], s[15])};
            o = MFMA32(__builtin_bit_cast(bf16x8, ld16(rws, cb + voff + T * 1536)), __builtin_bit_cast(bf16x8, pa), o);
            o = MFMA32(__builtin_bit_cast(bf16x8, ld16(rws, cb + voff + T * 1536 + 768)), __builtin_bit_cast(bf16x8, pb), o);
        }
        auto r = __builtin_amdgcn_permlane32_swap(__float_as_uint(o[8]), __float_as_uint(o[8]), false, false);
        lsum = __uint_as_float(r[1]);
    }
    const float rl = 1.f / lsum;
    const int q = qt * 32 + r32;
    const unsigned ob = OFF_O + (q >> 4) * 20480 + (q & 15) * 16;
    const int k0 = head * 20 + 4 * odd + 8 * hh, k1 = head * 20 + (odd ? 0 : 16);
    st16(rws, ob + (k0 >> 3) * 256, u32x4{pk(o[0] * rl, o[1] * rl), pk(o[2] * rl, o[3] * rl), pk(o[4] * rl, o[5] * rl), pk(o[6] * rl, o[7] * rl)});
    if (!hh) st8(rws, ob + (k1 >> 3) * 256 + (k1 & 4) * 2, u32x2{pk(o[8] * rl, o[9] * rl), pk(o[10] * rl, o[11] * rl)});
}

template <int NE>
DI void dft_body(unsigned lds0, int wv, int e0, rsrc_t rc, rsrc_t rws, unsigned csrc, unsigned absrc, int kstart, unsigned ydst) {
    const int l = threadIdx.x & 63, hh = l >> 5, r32 = l & 31, wm = wv & 3;
    const unsigned lane16 = l * 16;
    const unsigned coff = (unsigned)((8 * wv + (l >> 3)) * 4096 + (((l & 7) ^ ((4 * wv + (l >> 4)) & 7)) << 4));
    auto piece = [&](int i, int slot, int ks) {
        const unsigned base = lds0 + slot * 36864 + wv * 1024;
        const unsigned as = absrc + ks * 20480 + wv * 1024;
        if (i == 0) dma16(base, coff, rc, csrc + ks * 128);
        else if (i == 1) dma16(base + 8192, coff, rc, csrc + ks * 128 + 64 * 4096);
        else if (i == 2) dma16c(base + 16384, lane16, rws, as);
        else if (i == 3) dma16c(base + 16384 + 8192, lane16, rws, as + 8192);
        else if (NE == 2) dma16c(base + 16384 + 12288, lane16, rws, as + 12288);
    };
    int ksn = kstart;
#pragma unroll
    for (int i = 0; i < 5; ++i) piece(i, 0, ksn);
    const int row = wm * 32 + r32;
    const unsigned crd = lds0 + row * 128 + ((hh ^ ((row >> 1) & 7)) << 4);
    const unsigned ard = lds0 + 16384 + e0 * 1024 + lane16;
    f32x16 acc[NE];
#pragma unroll
    for (int e = 0; e < NE; ++e)
#pragma unroll
        for (int i = 0; i < 16; ++i) acc[e][i] = 0.f;
    bf16x8 cf[2], af[2][NE];
    const bf16x8 z8 = {0, 0, 0, 0, 0, 0, 0, 0};
    cf[1] = z8;
#pragma unroll
    for (int e = 0; e < NE; ++e) af[1][e] = z8;
    auto quarter = [&](int kk, unsigned so, bool pre, int slot) {
#pragma unroll
        for (int e = 0; e < NE; ++e) {
            acc[e] = MFMA32(af[(kk + 1) & 1][e], cf[(kk + 1) & 1], acc[e]);
            SB();
            if (e == 0) cf[kk & 1] = lds_rd16((crd ^ (kk << 5)) + so);
            af[kk & 1][e] = lds_rd16(ard + so + (kk * 5 + e) * 1024);
            if (pre && e < 2 && kk * 2 + e < 5) piece(kk * 2 + e, slot, ksn);
            SB();
        }
    };
#pragma unroll 2
    for (int ks = 0; ks < 16; ++ks) {
        WAIT_VM_LGKM0(0);
        BAR();
        if (ks == DFT_HI) __builtin_amdgcn_s_setprio(DFT_PRIO);
        ksn = (ksn + 1) & 15;
        const unsigned so = (ks & 1) * 36864;
        const bool pre = ks + 1 < 16;
        SB();
#pragma unroll
        for (int kk = 0; kk < 4; ++kk) quarter(kk, so, pre, (ks + 1) & 1);
    }
    __builtin_amdgcn_s_setprio(G_PRIO);
#pragma unroll
    for (int e = 0; e < NE; ++e) acc[e] = MFMA32(af[1][e], cf[1], acc[e]);
#pragma unroll
    for (int e = 0; e < NE; ++e)
#pragma unroll
        for (int s = 0; s < 2; ++s) {
            const u32x4 v = {pk(acc[e][8 * s], acc[e][8 * s + 1]), pk(acc[e][8 * s + 2], acc[e][8 * s + 3]),
                             pk(acc[e][8 * s + 4], acc[e][8 * s + 5]), pk(acc[e][8 * s + 6], acc[e][8 * s + 7])};
            st16(rws, ydst + ((e0 + e) * 2 + s) * 1024 + lane16, v);
        }
}

DI void final_phase(int mt, int nt, const bf16* wo, char* ws, bf16* out, unsigned lds0, int wv, u64 tag, int bid) {
    const int l = threadIdx.x & 63, kq = wv & 3, th = wv >> 2, i16 = l & 15, g = l >> 4;
    const rsrc_t rwo = mk_rsrc(wo, D_ * D_ * 2), rws = mk_rsrc(ws, WS_BYTES);
    unsigned woff[5];
#pragma unroll
    for (int i = 0; i < 5; ++i) { const int c = i * 64 + l, rho = c / 10; woff[i] = (unsigned)(swap23(rho) * (D_ * 2) + (c - rho * 10) * 16); }
    const unsigned wsrc = (unsigned)(kq * 160 * (D_ * 2) + nt * 160);
    const unsigned wrd = lds0 + kq * 20480 + ((g >> 1) * 16 + (g & 1) * 4 + (i16 >> 2)) * 160 + 8 * (l & 3);
#pragma unroll
    for (int i = 0; i < 3; ++i)
        if (th == 0 || i < 2) dma16(lds0 + kq * 20480 + (th * 3 + i) * 1024, th ? woff[3 + (i & 1)] : woff[i], rwo, wsrc + 128 * (D_ * 2));
    WAIT_VM(0);
    BAR();
    bf16x8 w4[5];
#pragma unroll
    for (int e = 0; e < 5; ++e) w4[e] = lds_tr2(wrd + e * 32, wrd + e * 32 + 1280);
    asm volatile("s_waitcnt lgkmcnt(0)\n\ts_barrier" ::: "memory");
#pragma unroll
    for (int b = 0; b < 2; ++b)
#pragma unroll
        for (int i = 0; i < 5; ++i) dma16(lds0 + kq * 20480 + (th * 2 + b) * 5120 + i * 1024, woff[i], rwo, wsrc + (th * 2 + b) * 32 * (D_ * 2));
    await2(ws, OFF_FG2 + (((mt >> 1) * 4 + (nt >> 1)) * 4 + (l & 3)) * 8, OFF_FG1 + 2048 + (mt * 8 + (l & 7)) * 8, tag, wv);
    STAMP(ws, 6);
    u32x4 yq[2]; float gv[2];
#pragma unroll
    for (int it = 0; it < 2; ++it) {
        if (it == 1 && wv >= 2) break;
        const int item = wv + 8 * it, t32 = item / 5, e16 = item - t32 * 5;
        u32x4 yv[4];
#pragma unroll
        for (int p = 0; p < 4; ++p) yv[p] = ld16(rws, OFF_Y + (unsigned)(((p * 64 + mt * 2 + t32) * 40 + nt * 5 + e16) * 1024 + l * 16));
        gv[it] = __uint_as_float(__builtin_amdgcn_raw_buffer_load_b32(rws, OFF_G + (mt * 64 + t32 * 32 + (l & 31)) * 4, 0, 16));
#pragma unroll
        for (int j = 0; j < 4; ++j)
            yq[it][j] = pk((bf_lo(yv[0][j]) + bf_lo(yv[1][j])) + (bf_lo(yv[2][j]) + bf_lo(yv[3][j])),
                           (bf_hi(yv[0][j]) + bf_hi(yv[1][j])) + (bf_hi(yv[2][j]) + bf_hi(yv[3][j])));
        PIN(yq[it]);
    }
    WAIT_VM(0);
    STAMP(ws, 7);
    BAR();
    await_wave(ws, OFF_FA2 + ((mt * 2 + th) * 32 + kq * 8 + (l & 7)) * 8, tag);
    STAMP(ws, 8);
    bf16x8 of[5][2];
#pragma unroll
    for (int k = 0; k < 5; ++k)
#pragma unroll
        for (int t = 0; t < 2; ++t)
            of[k][t] = __builtin_bit_cast(bf16x8, ld16(rws, OFF_O + (unsigned)(((mt * 4 + th * 2 + t) * 20 + kq * 5 + k) * 1024 + l * 16)));
    SB();
    f32x4 acc[5][2];
#pragma unroll
    for (int e = 0; e < 5; ++e) { acc[e][0] = f32x4{0.f, 0.f, 0.f, 0.f}; acc[e][1] = f32x4{0.f, 0.f, 0.f, 0.f}; }
#pragma unroll
    for (int k = 0; k < 5; ++k)
#pragma unroll
        for (int e = 0; e < 5; ++e) {
            const bf16x8 w = k < 4 ? lds_tr2(wrd + k * 5120 + e * 32, wrd + k * 5120 + e * 32 + 1280) : w4[e];
#pragma unroll
            for (int t = 0; t < 2; ++t) acc[e][t] = MFMA16(w, of[k][t], acc[e][t]);
        }
    asm volatile("s_waitcnt lgkmcnt(0)\n\ts_barrier" ::: "memory");
#pragma unroll
    for (int e = 0; e < 5; ++e)
#pragma unroll
        for (int t = 0; t < 2; ++t)
            *(LDSP f32x4*)(lds0 + (kq * 64 + th * 32 + t * 16 + i16) * 320 + e * 64 + ((g ^ (i16 >> 2)) << 4)) = acc[e][t];
    asm volatile("s_waitcnt lgkmcnt(0)\n\ts_barrier" ::: "memory");
#pragma unroll
    for (int it = 0; it < 2; ++it) {
        if (it == 1 && wv >= 2) break;
        const int item = wv + 8 * it, t32 = item / 5, e16 = item - t32 * 5;
        const int tl = t32 * 32 + (l & 31), sw = (l >> 2) & 3, c0 = (l >> 5) * 2;
        f32x4 s0 = {0.f, 0.f, 0.f, 0.f}, s1 = {0.f, 0.f, 0.f, 0.f};
#pragma unroll
        for (int p = 0; p < 4; ++p) {
            s0 += *(const LDSP f32x4*)(lds0 + (p * 64 + tl) * 320 + e16 * 64 + ((c0 ^ sw) << 4));
            s1 += *(const LDSP f32x4*)(lds0 + (p * 64 + tl) * 320 + e16 * 64 + (((c0 + 1) ^ sw) << 4));
        }
        const float gg = gv[it], g1 = 1.f - gg;
        const u32x4 y = yq[it];
        const u32x4 v = {pk(gg * bf_lo(y[0]) + g1 * s0[0], gg * bf_hi(y[0]) + g1 * s0[1]), pk(gg * bf_lo(y[1]) + g1 * s0[2], gg * bf_hi(y[1]) + g1 * s0[3]),
                         pk(gg * bf_lo(y[2]) + g1 * s1[0], gg * bf_hi(y[2]) + g1 * s1[1]), pk(gg * bf_lo(y[3]) + g1 * s1[2], gg * bf_hi(y[3]) + g1 * s1[3])};
        *(u32x4*)((char*)out + (unsigned)((mt * 64 + tl) * (D_ * 2) + (nt * 80 + e16 * 16 + (l >> 5) * 8) * 2)) = v;
    }
}

template <int PH>
__global__ __launch_bounds__(512, 4) void fused_kernel(const bf16* x, const bf16* cr, const bf16* ci, const bf16* wr, const bf16* wi,
                                                       const bf16* wqkv, const bf16* wo, const bf16* gw, char* ws, bf16* out) {
    extern __shared__ __attribute__((aligned(16))) char smem[];
    const unsigned lds0 = (unsigned)(uintptr_t)(LDSP char*)smem;
    const int bid = blockIdx.x ^ (SWAP_ROLES << 8), b = bid & 255, xcd = b & 7, slot = b >> 3, l = threadIdx.x & 63;
    const int wv = __builtin_amdgcn_readfirstlane(threadIdx.x >> 6);
    u64 tag = 0;
    {
        const u64 e = __hip_atomic_load((gu64*)(ws + OFF_EPOCH), RLX_AGENT) + 1;
        tag = ((u64)(unsigned)__builtin_amdgcn_readfirstlane((unsigned)(e >> 32)) << 32) | (unsigned)__builtin_amdgcn_readfirstlane((unsigned)e);
    }
    const int mt1 = (xcd & 1) * 8 + (slot & 7), nt1 = (xcd >> 1) * 4 + (slot >> 3);
    if (PH & 1) STAMP(ws, 0);
    if (bid < 256) {
        if (PH & 1) {
            __builtin_amdgcn_s_setprio(A_PRIO);
            for (int i = 0; i < A_DELAY; ++i) __builtin_amdgcn_s_sleep(16);
            if (nt1 < 10) qkv_body<1>(x, wqkv, mt1 * 128, nt1, ws, lds0, wv, bid);
            else if (nt1 < 15) qkv_body<2>(x, wqkv, mt1 * 128, nt1, ws, lds0, wv, bid);
            publish(ws, OFF_FA1 + (nt1 * 16 + mt1) * 8, tag);
            STAMP(ws, 2);
        }
        if (PH & 2) {
            const int head = xcd * 4 + (slot >> 3), qb = slot & 7;
            const int n0 = ((head >> 1) * 40) >> 7, n1 = ((head >> 1) * 40 + 39) >> 7;
            const int sec = l >> 4;
            const int mrow = sec == 1 || sec == 2 ? (l & 15) : qb * 2 + (l & 1);
            const int sb = sec == 3 ? 0 : sec * 5;
            await2(ws, OFF_FA1 + ((sb + n0) * 16 + mrow) * 8, OFF_FA1 + ((sb + n1) * 16 + mrow) * 8, tag, wv);
            STAMP(ws, 3);
            __builtin_amdgcn_s_setprio(ATT_PRIO);
            attn_phase(head, qb, ws, lds0, wv, bid);
            publish_wave(ws, OFF_FA2 + ((qb * 8 + wv) * 32 + head) * 8, tag);
            STAMP(ws, 9);
        }
    } else {
        const rsrc_t rws = mk_rsrc(ws, WS_BYTES);
        __builtin_amdgcn_s_setprio(G_PRIO);
        if (PH & 1) {
            {
                const int id = slot * 8 + wv;
                const unsigned* px = (const unsigned*)x + ((xcd & 1) * 10240 + id * 40 + l) * 32;
                const int wl = id * 20 + (l - 40);
                int wc = (xcd >> 1) * 8 + (wl & 7); wc = wc > 29 ? 29 : wc;
                const unsigned* pw = (const unsigned*)wqkv + (wl >> 3) * 960 + wc * 32;
                unsigned t0 = l < 60 ? *(l < 40 ? px : pw) : 0u;
                const int dl = id * 13 + l;
                unsigned t1 = (l < 13 && dl < 3200) ? ((const unsigned*)(xcd >= 4 ? wi : wr))[(((xcd >> 1) & 1) * 3200 + dl) * 32] : 0u;
                asm volatile("" :: "v"(t0), "v"(t1));
            }
            for (int i = 0; i < G_DELAY; ++i) __builtin_amdgcn_s_sleep(16);
            const bool isb = nt1 >= 8;
            if (wv < 4) ab_body<0>(x, isb ? wi : wr, isb, mt1 * 128, (nt1 & 7) * 80, ws, lds0, wv, bid);
            else ab_body<1>(x, isb ? wi : wr, isb, mt1 * 128, (nt1 & 7) * 80, ws, lds0, wv, bid);
            publish(ws, OFF_FG1 + (nt1 * 16 + mt1) * 8, tag);
            STAMP(ws, 2);
            const int row = b * 8 + wv;
            const unsigned* xr = (const unsigned*)(x + row * D_);
            const unsigned* gp = (const unsigned*)gw;
            unsigned xa[5], ga[5];
#pragma unroll
            for (int i = 0; i < 5; ++i) { xa[i] = xr[l + 64 * i]; ga[i] = gp[l + 64 * i]; }
            float s = 0.f;
#pragma unroll
            for (int i = 0; i < 5; ++i) s += bf_lo(xa[i]) * bf_lo(ga[i]) + bf_hi(xa[i]) * bf_hi(ga[i]);
#pragma unroll
            for (int off = 32; off > 0; off >>= 1) s += __shfl_xor(s, off);
            if (l == 0) st4(rws, OFF_G + row * 4, __float_as_uint(1.f / (1.f + __expf(-s))));
        }
        if (PH & 2) {
            const int kq = xcd & 3, mt = (xcd >> 2) * 8 + (slot >> 2), ntile = slot & 3;
            const unsigned fo = OFF_FG1 + (((kq >> 1) * 8 + ntile * 2) * 16 + (kq & 1) * 8 + (l & 7)) * 8;
            await2(ws, fo, fo + 128, tag, wv);
            STAMP(ws, 3);
            const rsrc_t rc = mk_rsrc(kq < 2 ? cr : ci, S_ * S_ * 2);
            const unsigned csrc = (unsigned)(mt * 128 * 4096 + (kq & 1) * 2048);
            const unsigned absrc = OFF_AB + (unsigned)((ntile * 64 + kq * 16) * 20480);
            const unsigned ydst = OFF_Y + (unsigned)(((kq * 64 + mt * 4 + (wv & 3)) * 40 + ntile * 10) * 1024);
            const int kstart = (2 * (slot >> 2) + 4 * ntile + (ntile & 1)) & 15;
            if (wv < 4) dft_body<3>(lds0, wv, 0, rc, rws, csrc, absrc, kstart, ydst);
            else dft_body<2>(lds0, wv, 3, rc, rws, csrc, absrc, kstart, ydst);
            publish(ws, OFF_FG2 + ((mt * 4 + ntile) * 4 + kq) * 8, tag);
            if (threadIdx.x == 0) __hip_atomic_store((gu64*)(ws + OFF_FG1 + 2048 + b * 8), tag, RLX_AGENT);
            STAMP(ws, 5);
        }
        if (PH & 4) {
            final_phase(xcd * 4 + (slot >> 3), slot & 7, wo, ws, out, lds0, wv, tag, bid);
            STAMP(ws, 9);
            if (b == 0 && wv == 0) {
                for (unsigned spins = 0; spins < (1u << 22); ++spins) {
                    bool ok = true;
#pragma unroll
                    for (int i = 0; i < 4; ++i)
                        ok = ok && __hip_atomic_load((gu64*)(ws + OFF_FA1 + (i * 64 + l) * 8), RLX_AGENT) == tag &&
                             __hip_atomic_load((gu64*)(ws + OFF_FG1 + (i * 64 + l) * 8), RLX_AGENT) == tag;
                    if (__all(ok)) break;
                    __builtin_amdgcn_s_sleep(2);
                }
                if (l == 0) __hip_atomic_store((gu64*)(ws + OFF_EPOCH), tag, RLX_AGENT);
            }
        }
    }
}

extern "C" void kernel_launch(void* const* d_in, const int* in_sizes, int n_in, void* d_out, int out_size, void* d_ws,
                              size_t ws_size, hipStream_t stream) {
    const bf16* x = (const bf16*)d_in[0];
    const bf16* cr = (const bf16*)d_in[1];
    const bf16* ci = (const bf16*)d_in[2];
    const bf16* wr = (const bf16*)d_in[3];
    const bf16* wi = (const bf16*)d_in[4];
    const bf16* wqkv = (const bf16*)d_in[6];
    const bf16* wo = (const bf16*)d_in[7];
    const bf16* gw = (const bf16*)d_in[8];
    char* ws = (char*)d_ws;
    bf16* out = (bf16*)d_out;
    (void)hipFuncSetAttribute((const void*)fused_kernel<7>, hipFuncAttributeMaxDynamicSharedMemorySize, LDS_BYTES);
    fused_kernel<7><<<512, 512, LDS_BYTES, stream>>>(x, cr, ci, wr, wi, wqkv, wo, gw, ws, out);
}
```

```cpp
#include <hip/hip_runtime.h>
#include <hip/hip_bf16.h>
#include <stdint.h>

#define FUSED 1

typedef short bf16x8 __attribute__((ext_vector_type(8)));
typedef short s16x4 __attribute__((ext_vector_type(4)));
typedef float f32x16 __attribute__((ext_vector_type(16)));
typedef float f32x4 __attribute__((ext_vector_type(4)));
typedef float f32x2 __attribute__((ext_vector_type(2)));
typedef unsigned u32x4 __attribute__((ext_vector_type(4)));
typedef unsigned u32x2 __attribute__((ext_vector_type(2)));
typedef __bf16 bf16x2_t __attribute__((ext_vector_type(2)));
typedef unsigned long long u64;
typedef __attribute__((address_space(1))) u64 gu64;
typedef __hip_bfloat16 bf16;
#define LDSP __attribute__((address_space(3)))
#define DI __device__ __forceinline__
typedef __amdgpu_buffer_rsrc_t rsrc_t;

constexpr int S_ = 2048, D_ = 640, H_ = 32;
constexpr unsigned OFF_EPOCH = 0;
constexpr unsigned OFF_FA1 = 16384, OFF_FG1 = 32768, OFF_FG2 = 49152, OFF_FG3 = 65536, OFF_FA2 = 131072;
constexpr unsigned OFF_G = 262144;
constexpr unsigned OFF_Q1 = 524288;
constexpr unsigned OFF_Q2 = OFF_Q1 + H_ * 65536;
constexpr unsigned OFF_K2 = OFF_Q2 + 16 * 32768;
constexpr unsigned KV_CHUNK = 18432, KV_HEAD = 8 * KV_CHUNK;
constexpr unsigned OFF_KV = OFF_K2 + 16 * 32768;
constexpr unsigned OFF_AB = OFF_KV + H_ * KV_HEAD;
constexpr unsigned OFF_Y = OFF_AB + 4 * 64 * 20480;
constexpr unsigned OFF_OM = OFF_Y + 4 * S_ * D_ * 2;
constexpr unsigned OFF_OL = OFF_OM + S_ * 512 * 2;
constexpr unsigned WS_BYTES = OFF_OL + H_ * S_ * 8;
constexpr int LDS_BYTES = 81920;
constexpr unsigned SPIN_CAP = 1u << 16;

#define STAMP(ws, i) do { } while (0)
#define TDECL
#define TREC(i) do { } while (0)
#define TDUMP(ws, n) do { } while (0)
#define MFMA32(a, b, c) __builtin_amdgcn_mfma_f32_32x32x16_bf16((a), (b), (c), 0, 0, 0)
#define MFMA16(a, b, c) __builtin_amdgcn_mfma_f32_16x16x32_bf16((a), (b), (c), 0, 0, 0)
#define WAIT_VM(n) asm volatile("s_waitcnt vmcnt(" #n ")" ::: "memory")
#define WAIT_VM_LGKM0(n) asm volatile("s_waitcnt vmcnt(" #n ") lgkmcnt(0)" ::: "memory")
#define BAR() asm volatile("s_barrier" ::: "memory")
#define EX2(v) __builtin_amdgcn_exp2f(v)
#define PIN(v) asm volatile("" : "+v"(v))
#define SB() __builtin_amdgcn_sched_barrier(0)
#define RLX_AGENT __ATOMIC_RELAXED, __HIP_MEMORY_SCOPE_AGENT

DI unsigned pk(float a, float b) { f32x2 v = {a, b}; return __builtin_bit_cast(unsigned, __builtin_convertvector(v, bf16x2_t)); }
DI float bf_lo(unsigned u) { return __uint_as_float(u << 16); }
DI float bf_hi(unsigned u) { return __uint_as_float(u & 0xffff0000u); }
DI rsrc_t mk_rsrc(const void* p, unsigned bytes) { return __builtin_amdgcn_make_buffer_rsrc((void*)p, (short)0, (int)bytes, 0x00020000); }
DI void dma16(unsigned lds_addr, unsigned voff, rsrc_t rs, unsigned soff) {
    asm volatile("s_mov_b32 m0, %0\n\ts_nop 0\n\tbuffer_load_dwordx4 %1, %2, %3 offen lds" ::"s"(lds_addr), "v"(voff), "s"(rs), "s"(soff) : "memory");
}
DI void dma16c(unsigned lds_addr, unsigned voff, rsrc_t rs, unsigned soff) {
    asm volatile("s_mov_b32 m0, %0\n\ts_nop 0\n\tbuffer_load_dwordx4 %1, %2, %3 offen sc1 lds" ::"s"(lds_addr), "v"(voff), "s"(rs), "s"(soff) : "memory");
}
DI void touch(unsigned lds_addr, unsigned voff, rsrc_t rs) {
    asm volatile("s_mov_b32 m0, %0\n\ts_nop 0\n\tbuffer_load_dword %1, %2, 0 offen lds" ::"s"(lds_addr), "v"(voff), "s"(rs) : "memory");
}
DI bf16x8 lds_rd16(unsigned a) { return *(const LDSP bf16x8*)(a); }
DI s16x4 lds_tr(unsigned a) { return __builtin_amdgcn_ds_read_tr16_b64_v4i16((LDSP s16x4*)(a)); }
DI bf16x8 lds_tr2(unsigned a0, unsigned a1) { return __builtin_shufflevector(lds_tr(a0), lds_tr(a1), 0, 1, 2, 3, 4, 5, 6, 7); }
DI void st16(rsrc_t rs, unsigned off, u32x4 v) { __builtin_amdgcn_raw_buffer_store_b128(v, rs, off, 0, 16); }
DI void st8(rsrc_t rs, unsigned off, u32x2 v) { __builtin_amdgcn_raw_buffer_store_b64(v, rs, off, 0, 16); }
DI void st4(rsrc_t rs, unsigned off, unsigned v) { __builtin_amdgcn_raw_buffer_store_b32(v, rs, off, 0, 16); }
DI u32x4 ld16(rsrc_t rs, unsigned off) { return __builtin_amdgcn_raw_buffer_load_b128(rs, off, 0, 16); }
DI int swap23(int i) { return (i & ~12) | ((i & 4) << 1) | ((i & 8) >> 1); }
DI u32x4 pack8(const f32x4& a, const f32x4& b) { return u32x4{pk(a[0], a[1]), pk(a[2], a[3]), pk(b[0], b[1]), pk(b[2], b[3])}; }

#define POLL_SLEEP 4
#define G_DELAY 9
#define A_DELAY 0
#define G_PRIO 2
#define DFT_PRIO 0
#define A_PRIO 3
#define ATT_PRIO 0
#define CODE_PF 39936
#define WO_PF 1
#define G_DELAY0 0
#define ROT 1
#define WARM 2
#define DFT_HI 10
#define SWAP_ROLES 0
DI u64 epoch_load(char* ws) {
    return __hip_atomic_load((gu64*)(ws + OFF_EPOCH), RLX_AGENT);
}
DI u64 epoch_tag(u64 e) {
    return (((u64)(unsigned)__builtin_amdgcn_readfirstlane((unsigned)(e >> 32)) << 32) | (unsigned)__builtin_amdgcn_readfirstlane((unsigned)e)) + 1;
}
DI void publish(char* ws, unsigned flag_off, u64 tag) {
    WAIT_VM(0);
    BAR();
    if (threadIdx.x < 8) __hip_atomic_store((gu64*)(ws + flag_off + threadIdx.x * 8), tag, RLX_AGENT);
}
DI void publish_wave(char* ws, unsigned flag_off, u64 tag) {
    WAIT_VM(0);
    if ((threadIdx.x & 63) < 8) __hip_atomic_store((gu64*)(ws + flag_off + (threadIdx.x & 63) * 8), tag, RLX_AGENT);
}
DI void await_wave(char* ws, unsigned off, u64 tag) {
    _Pragma("unroll 1") for (unsigned spins = 0; spins < SPIN_CAP; ++spins) {
        if (__all(__hip_atomic_load((gu64*)(ws + off), RLX_AGENT) == tag)) break;
        __builtin_amdgcn_s_sleep(POLL_SLEEP);
    }
    asm volatile("" ::: "memory");
}
DI void poll2(char* ws, unsigned off0, unsigned off1, u64 tag) {
    _Pragma("unroll 1") for (unsigned spins = 0; spins < SPIN_CAP; ++spins) {
        const u64 a = __hip_atomic_load((gu64*)(ws + off0), RLX_AGENT), b = __hip_atomic_load((gu64*)(ws + off1), RLX_AGENT);
        if (__all(a == tag && b == tag)) break;
        __builtin_amdgcn_s_sleep(POLL_SLEEP);
    }
}
DI void await2(char* ws, unsigned off0, unsigned off1, u64 tag, int wv) {
    if (wv == 0) poll2(ws, off0, off1, tag);
    asm volatile("s_waitcnt lgkmcnt(0)\n\ts_barrier" ::: "memory");
}

template <int KIND>
DI u64 qkv_body(const bf16* x, const bf16* wqkv, int m0, int nt, int k0, char* ws, unsigned lds0, int wv, int bid) {
    const int l = threadIdx.x & 63, wn = wv & 3, wm = wv >> 2, i16 = l & 15, g = l >> 4;
    const rsrc_t rx = mk_rsrc(x, S_ * D_ * 2), rw = mk_rsrc(wqkv, D_ * 1920 * 2);
    unsigned xoff, woff;
    {
        const int rho = 8 * wv + (l >> 3), i = rho & 15;
        const int s = KIND == 2 ? (rho & ~31) + 8 * (i >> 2) + 4 * ((rho >> 4) & 1) + (i & 3) : rho;
        xoff = (unsigned)((m0 + s) * (D_ * 2) + (((l & 7) ^ (l >> 3)) << 4));
        const int k = 4 * wv + (l >> 4), f = (k & 3) | (((k >> 3) & 1) << 2);
        woff = (unsigned)(k * 3840 + nt * 256 + (((i16 >> 1) ^ f) << 5) + ((i16 & 1) << 4));
    }
    auto piece = [&](int u, int i) {
        const unsigned dst = lds0 + (u % 5) * 16384 + i * 8192 + wv * 1024;
        const int ks = k0 + (u >> 1) - (k0 + (u >> 1) >= 10 ? 10 : 0);
        if (u & 1) dma16(dst, woff, rw, (ks * 64 + i * 32) * 3840);
        else dma16(dst, xoff, rx, ks * 128 + i * 64 * (D_ * 2));
    };
    TDECL; TREC(0);
#pragma unroll
    for (int u = 0; u < 5; ++u) { piece(u, 0); piece(u, 1); if (u == 1) BAR(); }
    const unsigned xrd = lds0 + (wm * 64 + i16) * 128 + ((g ^ (i16 & 7)) << 4);
    const unsigned wrd = lds0 + (8 * g + (i16 >> 2)) * 256 + (((2 * wn) ^ ((i16 >> 2) | ((g & 1) << 2))) << 5) + 8 * (l & 3);
    f32x4 acc[4][2];
#pragma unroll
    for (int m = 0; m < 4; ++m) { acc[m][0] = f32x4{0.f, 0.f, 0.f, 0.f}; acc[m][1] = f32x4{0.f, 0.f, 0.f, 0.f}; }
    bf16x8 xf[2][4], wf[2][2];
    u64 ep;
    auto rd1 = [&](int kt, int kk, int i) {
        const unsigned xs = ((2 * kt) % 5) * 16384, wsl = ((2 * kt + 1) % 5) * 16384;
        if (i < 2) { const unsigned a = (wrd ^ (i << 5)) + wsl + kk * 8192; wf[kk][i] = lds_tr2(a, a + 1024); }
        else if (i < 6) xf[kk][i - 2] = lds_rd16((xrd ^ (kk << 6)) + xs + (i - 2) * 2048);
    };
    auto mm = [&](int kk, int m) {
#pragma unroll
        for (int j = 0; j < 2; ++j) acc[m][j] = (KIND == 1) ? MFMA16(wf[kk][j], xf[kk][m], acc[m][j]) : MFMA16(xf[kk][m], wf[kk][j], acc[m][j]);
    };
#pragma unroll
    for (int kt = 0; kt < 10; ++kt) {
        TREC(3 * kt + 1);
        if (kt == 0) WAIT_VM_LGKM0(6); else if (kt < 9) WAIT_VM_LGKM0(2); else WAIT_VM_LGKM0(0);
        TREC(3 * kt + 2);
        BAR();
        TREC(3 * kt + 3);
        if (kt == 4) ep = epoch_load(ws);
        SB();
#pragma unroll
        for (int m = 0; m < 4; ++m) {
            if (kt > 0) mm(1, m);
            SB();
            rd1(kt, 0, 2 * m); rd1(kt, 0, 2 * m + 1);
            if (kt > 0 && 2 * kt + 3 + (m >> 1) < 20) piece(2 * kt + 3 + (m >> 1), m & 1);
            SB();
        }
#pragma unroll
        for (int m = 0; m < 4; ++m) {
            mm(0, m);
            SB();
            rd1(kt, 1, 2 * m); rd1(kt, 1, 2 * m + 1);
            SB();
        }
    }
#pragma unroll
    for (int m = 0; m < 4; ++m) mm(1, m);
    TREC(31); TDUMP(ws, 32);
    STAMP(ws, 1);
    const rsrc_t rws = mk_rsrc(ws, WS_BYTES);
    if (KIND == 2) {
#pragma unroll
        for (int j = 0; j < 2; ++j) {
            const int col = (nt - 10) * 128 + wn * 32 + 16 * j + i16;
            const int h = col / 20, d = col - h * 20;
            const int vp = d < 8 ? d : (d < 18 ? d - 8 : d - 10), vh = (d >= 4 && d < 12) || d == 16 || d == 17;
            const unsigned base = OFF_KV + (unsigned)(h * KV_HEAD + 8192 + vp * 1024 + vh * 512);
#pragma unroll
            for (int u = 0; u < 2; ++u) {
                const int s = m0 + wm * 64 + 32 * u + 8 * g;
                st16(rws, base + (s >> 8) * KV_CHUNK + (s & 255) * 2, pack8(acc[2 * u][j], acc[2 * u + 1][j]));
            }
        }
    } else {
        const bool isq = nt < 5;
        const float sc = isq ? 0.22360679774997896f * 1.4426950408889634f : 1.f;
        const int pos = isq ? (l & 31) : swap23(l & 31);
#pragma unroll
        for (int j = 0; j < 2; ++j) {
            const int c = (isq ? nt : nt - 5) * 128 + wn * 32 + 16 * j + 8 * (l >> 5);
            const int pair = c / 40, oc = (c - pair * 40) >> 3;
            const int h = 2 * pair + (oc > 2), half = oc > 2 ? oc - 3 : oc;
#pragma unroll
            for (int u = 0; u < 2; ++u) {
                const f32x4 a = acc[2 * u][j] * sc, b = acc[2 * u + 1][j] * sc;
                const auto r0 = __builtin_amdgcn_permlane16_swap(pk(a[0], a[1]), pk(b[0], b[1]), false, false);
                const auto r1 = __builtin_amdgcn_permlane16_swap(pk(a[2], a[3]), pk(b[2], b[3]), false, false);
                const int tile = (m0 + wm * 64 + 32 * u) >> 5;
                unsigned off;
                if (oc == 2) off = (isq ? OFF_Q2 : OFF_K2) + pair * 32768 + (tile * 32 + pos) * 16;
                else if (isq) off = OFF_Q1 + h * 65536 + tile * 1024 + (half * 32 + pos) * 16;
                else off = OFF_KV + h * KV_HEAD + (tile >> 3) * KV_CHUNK + (tile & 7) * 1024 + (half * 32 + pos) * 16;
                st16(rws, off, u32x4{r0[0], r1[0], r0[1], r1[1]});
            }
        }
    }
    WAIT_VM(0);
    return ep;
}

template <int WN>
DI u64 ab_body(const bf16* x, const bf16* w, bool isb, int m0, int n0, int k0, char* ws, unsigned lds0, int wv, int bid) {
    constexpr int NJ = WN == 0 ? 3 : 2, STG = 26624;
    const int l = threadIdx.x & 63, wm = wv & 3, i16 = l & 15, g = l >> 4;
    const rsrc_t rx = mk_rsrc(x, S_ * D_ * 2), rw = mk_rsrc(w, D_ * D_ * 2);
    unsigned xoff, woff;
    {
        const int rho = 8 * wv + (l >> 3), i = rho & 15;
        const int s = (rho & ~31) + 8 * (i >> 2) + 4 * ((rho >> 4) & 1) + (i & 3);
        xoff = (unsigned)((m0 + s) * (D_ * 2) + (((l & 7) ^ (l >> 3)) << 4));
        woff = (unsigned)((n0 + rho) * (D_ * 2) + (((l & 7) ^ (l >> 3)) << 4));
    }
    const bool extra = WN == 1 && wv < 6;
    auto piece = [&](int kt, int i) {
        const unsigned dst = lds0 + (kt % 3) * STG + wv * 1024;
        const int ks = k0 + kt - (k0 + kt >= 10 ? 10 : 0);
        if (i < 2) dma16(dst + i * 8192, xoff, rx, ks * 128 + i * 64 * (D_ * 2));
        else if (i == 2) dma16(dst + 16384, woff, rw, ks * 128);
        else if (WN == 1) { if (extra) dma16(dst + 16384 + 4096, woff, rw, ks * 128 + 32 * (D_ * 2)); }
    };
#pragma unroll
    for (int st = 0; st < 2; ++st)
#pragma unroll
        for (int i = 0; i < 4; ++i) piece(st, i);
    const unsigned xrd = lds0 + (wm * 32 + i16) * 128 + ((g ^ (i16 & 7)) << 4);
    const unsigned wrd = lds0 + 16384 + (WN * 48 + i16) * 128 + ((g ^ (i16 & 7)) << 4);
    f32x4 acc[2][NJ];
#pragma unroll
    for (int j = 0; j < NJ; ++j) { acc[0][j] = f32x4{0.f, 0.f, 0.f, 0.f}; acc[1][j] = f32x4{0.f, 0.f, 0.f, 0.f}; }
    bf16x8 xf[2][2], wf[2][NJ];
    u64 ep;
    auto rd1 = [&](int kt, int kk, int i) {
        const unsigned so = (kt % 3) * STG;
        if (i < 2) xf[kk][i] = lds_rd16((xrd ^ (kk << 6)) + so + i * 2048);
        else if (i - 2 < NJ) wf[kk][i - 2] = lds_rd16((wrd ^ (kk << 6)) + so + (i - 2) * 2048);
    };
    auto mm = [&](int kk, int j) {
#pragma unroll
        for (int m = 0; m < 2; ++m) acc[m][j] = MFMA16(xf[kk][m], wf[kk][j], acc[m][j]);
    };
#pragma unroll
    for (int kt = 0; kt < 10; ++kt) {
        if (kt < 9) { if (extra) WAIT_VM_LGKM0(4); else WAIT_VM_LGKM0(3); } else WAIT_VM_LGKM0(0);
        BAR();
        if (kt == 4) ep = epoch_load(ws);
        SB();
#pragma unroll
        for (int j = 0; j < NJ; ++j) {
            if (kt > 0) mm(1, j);
            SB();
            rd1(kt, 0, 2 * j); rd1(kt, 0, 2 * j + 1);
            if (kt + 2 < 10 && j < 2) piece(kt + 2, j);
            SB();
        }
#pragma unroll
        for (int j = 0; j < NJ; ++j) {
            mm(0, j);
            SB();
            rd1(kt, 1, 2 * j); rd1(kt, 1, 2 * j + 1);
            if (kt + 2 < 10 && j < 2) piece(kt + 2, 2 + j);
            SB();
        }
    }
#pragma unroll
    for (int j = 0; j < NJ; ++j) mm(1, j);
    STAMP(ws, 1);
    const rsrc_t rws = mk_rsrc(ws, WS_BYTES);
    const float sg = isb ? -1.f : 1.f;
    const int kg = (isb ? 2048 : 0) + m0 + 32 * wm + 8 * g;
#pragma unroll
    for (int j = 0; j < NJ; ++j) {
        const int e = n0 + WN * 48 + 16 * j + i16;
        const int ntile = e / 160, er = e - ntile * 160;
        st16(rws, OFF_AB + (unsigned)(ntile * 64 * 20480 + (er >> 5) * 1024 + swap23(er & 31) * 16 + (kg >> 4) * 5120 + ((kg >> 3) & 1) * 512),
             pack8(acc[0][j] * sg, acc[1][j] * sg));
    }
    WAIT_VM(0);
    return ep;
}

constexpr int AT_SLOT = 23728;

DI float half_swap_max(float m) {
    auto r = __builtin_amdgcn_permlane32_swap(__float_as_uint(m), __float_as_uint(m), false, false);
    return fmaxf(__uint_as_float(r[0]), __uint_as_float(r[1]));
}
DI float max16(const f32x16& s) {
    float a = fmaxf(fmaxf(s[0], s[1]), s[2]), b = fmaxf(fmaxf(s[3], s[4]), s[5]);
    a = fmaxf(fmaxf(a, s[6]), s[7]); b = fmaxf(fmaxf(b, s[8]), s[9]);
    a = fmaxf(fmaxf(a, s[10]), s[11]); b = fmaxf(fmaxf(b, s[12]), s[13]);
    a = fmaxf(fmaxf(a, s[14]), s[15]);
    return fmaxf(a, b);
}

DI void attn_phase(int head, int qb, char* ws, unsigned lds0, int wv, u64 tag, unsigned f0, unsigned f1, int bid) {
    const int l = threadIdx.x & 63, hh = l >> 5, r32 = l & 31;
    const int qt = qb * 8 + wv, pair = head >> 1, odd = head & 1;
    const rsrc_t rws = mk_rsrc(ws, WS_BYTES);
    const unsigned lane16 = l * 16;
    const unsigned kvsrc = OFF_KV + head * KV_HEAD + wv * 1024;
    const unsigned src3 = wv < 2 ? kvsrc + 16384 : OFF_K2 + pair * 32768 + (wv - 2) * 1024, step3 = wv < 2 ? KV_CHUNK : 4096;
    const unsigned dst3 = wv < 2 ? 12288 + (8 + wv) * 1040 : 8192 + (wv - 2) * 1024;
    auto load_chunk = [&](int c, int slot) {
        const unsigned dst = lds0 + slot * AT_SLOT;
        const int cc = (c + qb) & 7;
        dma16c(dst + wv * 1024, lane16, rws, kvsrc + cc * KV_CHUNK);
        dma16c(dst + 12288 + wv * 1040, lane16, rws, kvsrc + cc * KV_CHUNK + 8192);
        if (wv < 6) dma16c(dst + dst3, lane16, rws, src3 + cc * step3);
    };
    load_chunk(0, 0);
    const bf16x8 q1 = __builtin_bit_cast(bf16x8, ld16(rws, OFF_Q1 + head * 65536 + qt * 1024 + lane16));
    const u32x4 qr = ld16(rws, OFF_Q2 + pair * 32768 + (qt * 32 + r32) * 16);
    load_chunk(1, 1);
    if (wv < 3) {
        const unsigned c = hh ? 0u : 0x3F803F80u;
        *(LDSP u32x4*)(lds0 + wv * AT_SLOT + 12288 + 10400 + lane16 + hh * 16) = u32x4{c, c, c, c};
    }
    bf16x8 q2[2];
#pragma unroll
    for (int P = 0; P < 2; ++P) {
        const bool lo = hh == P && !odd, hi = hh == P && odd;
        q2[P] = __builtin_bit_cast(bf16x8, u32x4{lo ? qr[0] : 0u, lo ? qr[1] : 0u, hi ? qr[2] : 0u, hi ? qr[3] : 0u});
    }
    const bool vdata = r32 < 18 || r32 == 20 || r32 == 21, vone = r32 == 18 || r32 == 22;
    const int vp = r32 < 4 ? r32 : (r32 < 12 ? r32 - 4 : (r32 < 18 ? r32 - 8 : r32 - 12)), vh = (r32 >= 4 && r32 < 12) || r32 == 16 || r32 == 17;
    const unsigned voff = 12288 + hh * 16 + (vdata ? vp * 1040 + vh * 512 : (vone ? 10400 : 10928));
    const f32x16 zero16 = {0.f, 0.f, 0.f, 0.f, 0.f, 0.f, 0.f, 0.f, 0.f, 0.f, 0.f, 0.f, 0.f, 0.f, 0.f, 0.f};
    WAIT_VM_LGKM0(2);
    BAR();
    STAMP(ws, 4);
    unsigned kb = lds0 + lane16, vb = lds0 + voff;
    bf16x8 k2f = lds_rd16(kb + 8192);
    f32x16 sc = MFMA32(lds_rd16(kb), q1, zero16);
    sc = MFMA32(k2f, q2[0], sc);
    const float m0 = half_swap_max(max16(sc));
    f32x16 negm;
#pragma unroll
    for (int i = 0; i < 16; ++i) { negm[i] = -m0; sc[i] -= m0; }
    f32x16 o = zero16;
    bf16x8 k1f = lds_rd16(kb + 1024);
    bf16x8 v0f = lds_rd16(vb), v1f = lds_rd16(vb);
    u32x4 p0, p1 = {0u, 0u, 0u, 0u};
    float e12 = 0.f, e13 = 0.f, e14 = 0.f, e15 = 0.f;
    int slot = 0;
    for (int c = 0; c < 8; ++c) {
        const int nslot = slot == 2 ? 0 : slot + 1;
        const unsigned kbn = lds0 + nslot * AT_SLOT + lane16, vbn = lds0 + nslot * AT_SLOT + voff;
#pragma unroll
        for (int t = 0; t < 8; ++t) {
            if (t == 6) {
                if (c == 0 && wv == 0) poll2(ws, f0, f1, tag);
                WAIT_VM(0);
                BAR();
                if (c + 2 < 8) load_chunk(c + 2, nslot == 2 ? 0 : nslot + 1);
            }
            float a0, a1, a2, a3, b0, b1, b2, b3;
            SB();
            f32x16 sn = MFMA32(k1f, q1, negm);
            k1f = (t < 6) ? lds_rd16(kb + (t + 2) * 1024) : lds_rd16(kbn + (t - 6) * 1024);
            p1[2] = pk(e12, e13); p1[3] = pk(e14, e15); PIN(p1);
            a0 = EX2(sc[0]); a1 = EX2(sc[1]); a2 = EX2(sc[2]); a3 = EX2(sc[3]); PIN(a0); PIN(a1); PIN(a2); PIN(a3);
            SB();
            sn = MFMA32(k2f, q2[(t + 1) & 1], sn);
            if ((t & 1) == 0) k2f = (t < 6) ? lds_rd16(kb + 8192 + (t + 2) * 512) : lds_rd16(kbn + 8192);
            p0[0] = pk(a0, a1); p0[1] = pk(a2, a3);
            b0 = EX2(sc[4]); b1 = EX2(sc[5]); b2 = EX2(sc[6]); b3 = EX2(sc[7]); PIN(b0); PIN(b1); PIN(b2); PIN(b3);
            SB();
            o = MFMA32(v1f, __builtin_bit_cast(bf16x8, p1), o);
            v1f = lds_rd16(vb + t * 64 + 32);
            p0[2] = pk(b0, b1); p0[3] = pk(b2, b3); PIN(p0);
            a0 = EX2(sc[8]); a1 = EX2(sc[9]); a2 = EX2(sc[10]); a3 = EX2(sc[11]); PIN(a0); PIN(a1); PIN(a2); PIN(a3);
            SB();
            o = MFMA32(v0f, __builtin_bit_cast(bf16x8, p0), o);
            v0f = (t < 7) ? lds_rd16(vb + (t + 1) * 64) : lds_rd16(vbn);
            p1[0] = pk(a0, a1); p1[1] = pk(a2, a3);
            e12 = EX2(sc[12]); e13 = EX2(sc[13]); e14 = EX2(sc[14]); e15 = EX2(sc[15]); PIN(e12); PIN(e13); PIN(e14); PIN(e15);
            SB();
            sc = sn;
        }
        slot = nslot; kb = kbn; vb = vbn;
    }
    p1[2] = pk(e12, e13); p1[3] = pk(e14, e15);
    o = MFMA32(v1f, __builtin_bit_cast(bf16x8, p1), o);
    STAMP(ws, 5);
    float asum = 0.f;
#pragma unroll
    for (int i = 0; i < 10; ++i) asum += fabsf(o[i]);
    const bool bad = !(o[10] <= 1.0e18f) || !(asum <= 1.0e37f);
    if (__builtin_expect(__any(bad), 0)) {
        const unsigned vg = 8192 + hh * 16 + (vdata ? vp * 1024 + vh * 512 : 0), vc = vone ? 0x3F803F80u : 0u;
        const u32x4 vconst = {vc, vc, vc, vc};
        float m = -INFINITY;
        o = zero16;
        for (int t = 0; t < 64; ++t) {
            const unsigned cb = OFF_KV + head * KV_HEAD + (t >> 3) * KV_CHUNK;
            const int T = t & 7;
            f32x16 s = MFMA32(__builtin_bit_cast(bf16x8, ld16(rws, cb + T * 1024 + lane16)), q1, zero16);
            s = MFMA32(__builtin_bit_cast(bf16x8, ld16(rws, OFF_K2 + pair * 32768 + (t * 32 + r32) * 16)), q2[0], s);
            const float mn = fmaxf(m, half_swap_max(max16(s)));
            const float al = EX2(m - mn);
            m = mn;
#pragma unroll
            for (int i = 0; i < 16; ++i) { o[i] *= al; s[i] = EX2(s[i] - mn); }
            const u32x4 pa = {pk(s[0], s[1]), pk(s[2], s[3]), pk(s[4], s[5]), pk(s[6], s[7])};
            const u32x4 pb = {pk(s[8], s[9]), pk(s[10], s[11]), pk(s[12], s[13]), pk(s[14], s[15])};
            const u32x4 va = ld16(rws, cb + vg + T * 64), vb2 = ld16(rws, cb + vg + T * 64 + 32);
            o = MFMA32(__builtin_bit_cast(bf16x8, vdata ? va : vconst), __builtin_bit_cast(bf16x8, pa), o);
            o = MFMA32(__builtin_bit_cast(bf16x8, vdata ? vb2 : vconst), __builtin_bit_cast(bf16x8, pb), o);
        }
    }
    const float rl = 1.f / o[10];
    const int q = qt * 32 + r32;
    st16(rws, OFF_OM + ((q >> 4) * 16 + pair) * 1024 + (odd * 2 + hh) * 256 + (q & 15) * 16,
         u32x4{pk(o[0] * rl, o[1] * rl), pk(o[2] * rl, o[3] * rl), pk(o[4] * rl, o[5] * rl), pk(o[6] * rl, o[7] * rl)});
    const unsigned xl = pk(o[8] * rl, o[9] * rl);
    const auto rx = __builtin_amdgcn_permlane32_swap(xl, xl, false, false);
    if (!hh) st8(rws, OFF_OL + (head * S_ + q) * 8, u32x2{rx[0], rx[1]});
}

template <int NE>
DI void dft_body(unsigned lds0, int wv, int e0, rsrc_t rc, rsrc_t rws, unsigned csrc, unsigned absrc, int kstart, unsigned ydst) {
    const int l = threadIdx.x & 63, hh = l >> 5, r32 = l & 31, wm = wv & 3;
    const unsigned lane16 = l * 16;
    const unsigned coff = (unsigned)((8 * wv + (l >> 3)) * 4096 + (((l & 7) ^ ((4 * wv + (l >> 4)) & 7)) << 4));
    auto piece = [&](int i, int slot, int ks) {
        const unsigned base = lds0 + slot * 36864 + wv * 1024;
        const unsigned as = absrc + ks * 20480 + wv * 1024;
        if (i == 0) dma16(base, coff, rc, csrc + ks * 128);
        else if (i == 1) dma16(base + 8192, coff, rc, csrc + ks * 128 + 64 * 4096);
        else if (i == 2) dma16c(base + 16384, lane16, rws, as);
        else if (i == 3) dma16c(base + 16384 + 8192, lane16, rws, as + 8192);
        else if (NE == 2) dma16c(base + 16384 + 12288, lane16, rws, as + 12288);
    };
    int ksn = kstart;
#pragma unroll
    for (int i = 0; i < 5; ++i) piece(i, 0, ksn);
    const int row = wm * 32 + r32;
    const unsigned crd = lds0 + row * 128 + ((hh ^ ((row >> 1) & 7)) << 4);
    const unsigned ard = lds0 + 16384 + e0 * 1024 + lane16;
    f32x16 acc[NE];
#pragma unroll
    for (int e = 0; e < NE; ++e)
#pragma unroll
        for (int i = 0; i < 16; ++i) acc[e][i] = 0.f;
    bf16x8 cf[2], af[2][NE];
    const bf16x8 z8 = {0, 0, 0, 0, 0, 0, 0, 0};
    cf[1] = z8;
#pragma unroll
    for (int e = 0; e < NE; ++e) af[1][e] = z8;
    auto quarter = [&](int kk, unsigned so, bool pre, int slot) {
#pragma unroll
        for (int e = 0; e < NE; ++e) {
            acc[e] = MFMA32(af[(kk + 1) & 1][e], cf[(kk + 1) & 1], acc[e]);
            SB();
            if (e == 0) cf[kk & 1] = lds_rd16((crd ^ (kk << 5)) + so);
            af[kk & 1][e] = lds_rd16(ard + so + (kk * 5 + e) * 1024);
            if (pre && e < 2 && kk * 2 + e < 5) piece(kk * 2 + e, slot, ksn);
            SB();
        }
    };
#pragma unroll 2
    for (int ks = 0; ks < 16; ++ks) {
        WAIT_VM_LGKM0(0);
        BAR();
        if (ks == DFT_HI) __builtin_amdgcn_s_setprio(DFT_PRIO);
        ksn = (ksn + 1) & 15;
        const unsigned so = (ks & 1) * 36864;
        const bool pre = ks + 1 < 16;
        SB();
#pragma unroll
        for (int kk = 0; kk < 4; ++kk) quarter(kk, so, pre, (ks + 1) & 1);
    }
    __builtin_amdgcn_s_setprio(G_PRIO);
#pragma unroll
    for (int e = 0; e < NE; ++e) acc[e] = MFMA32(af[1][e], cf[1], acc[e]);
#pragma unroll
    for (int e = 0; e < NE; ++e)
#pragma unroll
        for (int s = 0; s < 2; ++s) {
            const u32x4 v = {pk(acc[e][8 * s], acc[e][8 * s + 1]), pk(acc[e][8 * s + 2], acc[e][8 * s + 3]),
                             pk(acc[e][8 * s + 4], acc[e][8 * s + 5]), pk(acc[e][8 * s + 6], acc[e][8 * s + 7])};
            st16(rws, ydst + ((e0 + e) * 2 + s) * 1024 + lane16, v);
        }
}

DI void final_phase(int mt, int nt, const bf16* wo, char* ws, bf16* out, unsigned lds0, int wv, u64 tag, int bid) {
    const int l = threadIdx.x & 63, kq = wv & 3, th = wv >> 2, i16 = l & 15, g = l >> 4;
    const rsrc_t rwo = mk_rsrc(wo, D_ * D_ * 2), rws = mk_rsrc(ws, WS_BYTES);
    const unsigned wsrc = (unsigned)(kq * 160 * (D_ * 2) + nt * 160);
    const unsigned wrd = lds0 + kq * 20480 + ((g >> 1) * 16 + (g & 1) * 4 + (i16 >> 2)) * 160 + 8 * (l & 3);
#pragma unroll
    for (int i = 0; i < 3; ++i) {
        const int c = (th * 3 + i) * 64 + l, rho = c / 10, ka = swap23(rho);
        if (th == 0 || i < 2) dma16(lds0 + kq * 20480 + (th * 3 + i) * 1024, (unsigned)((20 * (ka >> 2) + 16 + (ka & 3)) * (D_ * 2) + (c - rho * 10) * 16), rwo, wsrc);
    }
    unsigned woff[5];
#pragma unroll
    for (int i = 0; i < 5; ++i) {
        const int c = i * 64 + l, rho = c / 10, ka = swap23(rho);
        woff[i] = (unsigned)((20 * (ka >> 4) + (ka & 15)) * (D_ * 2) + (c - rho * 10) * 16);
    }
    WAIT_VM(0);
    BAR();
    bf16x8 w4[5];
#pragma unroll
    for (int e = 0; e < 5; ++e) w4[e] = lds_tr2(wrd + e * 32, wrd + e * 32 + 1280);
    asm volatile("s_waitcnt lgkmcnt(0)\n\ts_barrier" ::: "memory");
#pragma unroll
    for (int b = 0; b < 2; ++b)
#pragma unroll
        for (int i = 0; i < 5; ++i) dma16(lds0 + kq * 20480 + (th * 2 + b) * 5120 + i * 1024, woff[i], rwo, wsrc + (th * 2 + b) * 40 * (D_ * 2));
    await2(ws, OFF_FG2 + (((mt >> 1) * 4 + (nt >> 1)) * 4 + (l & 3)) * 64, OFF_FG3 + (mt * 8 + (l & 7)) * 64, tag, wv);
    STAMP(ws, 6);
    u32x4 yq[2]; float gv[2];
#pragma unroll
    for (int it = 0; it < 2; ++it) {
        if (it == 1 && wv >= 2) break;
        const int item = wv + 8 * it, t32 = item / 5, e16 = item - t32 * 5;
        u32x4 yv[4];
#pragma unroll
        for (int p = 0; p < 4; ++p) yv[p] = ld16(rws, OFF_Y + (unsigned)(((p * 64 + mt * 2 + t32) * 40 + nt * 5 + e16) * 1024 + l * 16));
        gv[it] = __uint_as_float(__builtin_amdgcn_raw_buffer_load_b32(rws, OFF_G + (mt * 64 + t32 * 32 + (l & 31)) * 64, 0, 16));
#pragma unroll
        for (int j = 0; j < 4; ++j)
            yq[it][j] = pk((bf_lo(yv[0][j]) + bf_lo(yv[1][j])) + (bf_lo(yv[2][j]) + bf_lo(yv[3][j])),
                           (bf_hi(yv[0][j]) + bf_hi(yv[1][j])) + (bf_hi(yv[2][j]) + bf_hi(yv[3][j])));
        PIN(yq[it]);
    }
    WAIT_VM(0);
    STAMP(ws, 7);
    BAR();
    await_wave(ws, OFF_FA2 + ((mt * 2 + th) * 32 + kq * 8 + (l & 7)) * 64, tag);
    STAMP(ws, 8);
    bf16x8 of[5][2];
#pragma unroll
    for (int k = 0; k < 4; ++k)
#pragma unroll
        for (int t = 0; t < 2; ++t)
            of[k][t] = __builtin_bit_cast(bf16x8, ld16(rws, OFF_OM + (unsigned)(((mt * 4 + th * 2 + t) * 16 + kq * 4 + k) * 1024 + l * 16)));
#pragma unroll
    for (int t = 0; t < 2; ++t) {
        const unsigned off = OFF_OL + (unsigned)(((kq * 8 + 2 * g) * S_ + mt * 64 + th * 32 + t * 16 + i16) * 8);
        const u32x2 lo = __builtin_amdgcn_raw_buffer_load_b64(rws, off, 0, 16), hi = __builtin_amdgcn_raw_buffer_load_b64(rws, off + S_ * 8, 0, 16);
        of[4][t] = __builtin_bit_cast(bf16x8, u32x4{lo[0], lo[1], hi[0], hi[1]});
    }
    SB();
    f32x4 acc[5][2];
#pragma unroll
    for (int e = 0; e < 5; ++e) { acc[e][0] = f32x4{0.f, 0.f, 0.f, 0.f}; acc[e][1] = f32x4{0.f, 0.f, 0.f, 0.f}; }
#pragma unroll
    for (int k = 0; k < 5; ++k)
#pragma unroll
        for (int e = 0; e < 5; ++e) {
            const bf16x8 w = k < 4 ? lds_tr2(wrd + k * 5120 + e * 32, wrd + k * 5120 + e * 32 + 1280) : w4[e];
#pragma unroll
            for (int t = 0; t < 2; ++t) acc[e][t] = MFMA16(w, of[k][t], acc[e][t]);
        }
    asm volatile("s_waitcnt lgkmcnt(0)\n\ts_barrier" ::: "memory");
#pragma unroll
    for (int e = 0; e < 5; ++e)
#pragma unroll
        for (int t = 0; t < 2; ++t)
            *(LDSP f32x4*)(lds0 + (kq * 64 + th * 32 + t * 16 + i16) * 320 + e * 64 + ((g ^ (i16 >> 2)) << 4)) = acc[e][t];
    asm volatile("s_waitcnt lgkmcnt(0)\n\ts_barrier" ::: "memory");
#pragma unroll
    for (int it = 0; it < 2; ++it) {
        if (it == 1 && wv >= 2) break;
        const int item = wv + 8 * it, t32 = item / 5, e16 = item - t32 * 5;
        const int tl = t32 * 32 + (l & 31), sw = (l >> 2) & 3, c0 = (l >> 5) * 2;
        f32x4 s0 = {0.f, 0.f, 0.f, 0.f}, s1 = {0.f, 0.f, 0.f, 0.f};
#pragma unroll
        for (int p = 0; p < 4; ++p) {
            s0 += *(const LDSP f32x4*)(lds0 + (p * 64 + tl) * 320 + e16 * 64 + ((c0 ^ sw) << 4));
            s1 += *(const LDSP f32x4*)(lds0 + (p * 64 + tl) * 320 + e16 * 64 + (((c0 + 1) ^ sw) << 4));
        }
        const float gg = gv[it], g1 = 1.f - gg;
        const u32x4 y = yq[it];
        const u32x4 v = {pk(gg * bf_lo(y[0]) + g1 * s0[0], gg * bf_hi(y[0]) + g1 * s0[1]), pk(gg * bf_lo(y[1]) + g1 * s0[2], gg * bf_hi(y[1]) + g1 * s0[3]),
                         pk(gg * bf_lo(y[2]) + g1 * s1[0], gg * bf_hi(y[2]) + g1 * s1[1]), pk(gg * bf_lo(y[3]) + g1 * s1[2], gg * bf_hi(y[3]) + g1 * s1[3])};
        *(u32x4*)((char*)out + (unsigned)((mt * 64 + tl) * (D_ * 2) + (nt * 80 + e16 * 16 + (l >> 5) * 8) * 2)) = v;
    }
}

template <int PH>
__global__ __launch_bounds__(512, 4) void fused_kernel(const bf16* x, const bf16* cr, const bf16* ci, const bf16* wr, const bf16* wi,
                                                       const bf16* wqkv, const bf16* wo, const bf16* gw, char* ws, bf16* out) {
    extern __shared__ __attribute__((aligned(16))) char smem[];
    const unsigned lds0 = (unsigned)(uintptr_t)(LDSP char*)smem;
    const int bid = blockIdx.x ^ (SWAP_ROLES << 8), b = bid & 255, xcd = b & 7, slot = b >> 3, l = threadIdx.x & 63;
    const int wv = __builtin_amdgcn_readfirstlane(threadIdx.x >> 6);
    u64 tag = 0;
    const int mt1 = (xcd & 1) * 8 + (slot & 7), nt1 = (xcd >> 1) * 4 + (slot >> 3);
    const int k0 = ROT ? (2 * (slot & 7) + (((slot >> 3) * 5 + 1) >> 1)) % 10 : 0;
    const char* pc;
    asm volatile("s_getpc_b64 %0" : "=s"(pc));
    if (PH & 1) STAMP(ws, 0);
    if (__builtin_expect(bid < 256, 1)) {
        if (PH & 1) {
            __builtin_amdgcn_s_setprio(A_PRIO);
            for (int i = 0; i < A_DELAY; ++i) __builtin_amdgcn_s_sleep(16);
            u64 ep;
            if (nt1 < 10) ep = qkv_body<1>(x, wqkv, mt1 * 128, nt1, k0, ws, lds0, wv, bid);
            else if (nt1 < 15) ep = qkv_body<2>(x, wqkv, mt1 * 128, nt1, k0, ws, lds0, wv, bid);
            else ep = epoch_load(ws);
            tag = epoch_tag(ep);
            publish(ws, OFF_FA1 + (nt1 * 16 + mt1) * 64, tag);
            STAMP(ws, 2);
        }
        if (PH & 2) {
            const int head = xcd * 4 + (slot >> 3), qb = slot & 7;
            const int n0 = ((head >> 1) * 40) >> 7, n1 = ((head >> 1) * 40 + 39) >> 7;
            const int sec = (l >> 2) & 3, sb = sec == 3 ? 0 : sec * 5;
            const int mrow = (qb * 2 + (sec == 1 || sec == 2 ? (l & 3) : (l & 1))) & 15;
            await2(ws, OFF_FA1 + ((sb + n0) * 16 + mrow) * 64, OFF_FA1 + ((sb + n1) * 16 + mrow) * 64, tag, wv);
            STAMP(ws, 3);
            __builtin_amdgcn_s_setprio(ATT_PRIO);
            const int sb2 = 5 + ((l >> 4) & 1) * 5;
            attn_phase(head, qb, ws, lds0, wv, tag, OFF_FA1 + ((sb2 + n0) * 16 + (l & 15)) * 64, OFF_FA1 + ((sb2 + n1) * 16 + (l & 15)) * 64, bid);
            publish_wave(ws, OFF_FA2 + ((qb * 8 + wv) * 32 + head) * 64, tag);
            STAMP(ws, 9);
        }
    } else {
        const rsrc_t rws = mk_rsrc(ws, WS_BYTES);
        __builtin_amdgcn_s_setprio(G_PRIO);
        if (PH & 1) {
            {
                const unsigned td = lds0 + 79872 + wv * 256;
                if (wv == 0) touch(td, l < 10 ? (unsigned)((l * 32 + slot) * 128) : 0x7ffffff0u, mk_rsrc(pc, CODE_PF));
                const int id = slot * 8 + wv;
                const int dl = id * 13 + l;
                touch(td, l < 13 && dl < 3200 ? (unsigned)((((xcd >> 1) & 1) * 3200 + dl) * 128) : 0x7ffffff0u, mk_rsrc(xcd >= 4 ? wi : wr, D_ * D_ * 2));
            }
            for (int i = 0; i < G_DELAY0; ++i) __builtin_amdgcn_s_sleep(16);
            if (wv == 0) touch(lds0 + 79872, l < 25 ? (unsigned)((b * 25 + l) * 128) : 0x7ffffff0u, mk_rsrc(wo, D_ * D_ * 2));
            for (int i = G_DELAY0; i < G_DELAY; ++i) __builtin_amdgcn_s_sleep(16);
            const bool isb = nt1 >= 8;
            u64 ep;
            if (wv < 4) ep = ab_body<0>(x, isb ? wi : wr, isb, mt1 * 128, (nt1 & 7) * 80, k0, ws, lds0, wv, bid);
            else ep = ab_body<1>(x, isb ? wi : wr, isb, mt1 * 128, (nt1 & 7) * 80, k0, ws, lds0, wv, bid);
            tag = epoch_tag(ep);
            publish(ws, OFF_FG1 + (nt1 * 16 + mt1) * 64, tag);
            STAMP(ws, 2);
            const int row = b * 8 + wv;
            const unsigned* xr = (const unsigned*)(x + row * D_);
            const unsigned* gp = (const unsigned*)gw;
            unsigned xa[5], ga[5];
#pragma unroll
            for (int i = 0; i < 5; ++i) { xa[i] = xr[l + 64 * i]; ga[i] = gp[l + 64 * i]; }
            float s = 0.f;
#pragma unroll
            for (int i = 0; i < 5; ++i) s += bf_lo(xa[i]) * bf_lo(ga[i]) + bf_hi(xa[i]) * bf_hi(ga[i]);
#pragma unroll
            for (int off = 32; off > 0; off >>= 1) s += __shfl_xor(s, off);
            if (l < 16) st4(rws, OFF_G + row * 64 + l * 4, __float_as_uint(1.f / (1.f + __expf(-s))));
        }
        if (PH & 2) {
            const int kq = xcd & 3, mt = (xcd >> 2) * 8 + (slot >> 2), ntile = slot & 3;
            const unsigned fo = OFF_FG1 + (((kq >> 1) * 8 + ntile * 2) * 16 + (kq & 1) * 8 + (l & 7)) * 64;
            await2(ws, fo, fo + 1024, tag, wv);
            STAMP(ws, 3);
            const rsrc_t rc = mk_rsrc(kq < 2 ? cr : ci, S_ * S_ * 2);
            const unsigned csrc = (unsigned)(mt * 128 * 4096 + (kq & 1) * 2048);
            const unsigned absrc = OFF_AB + (unsigned)((ntile * 64 + kq * 16) * 20480);
            const unsigned ydst = OFF_Y + (unsigned)(((kq * 64 + mt * 4 + (wv & 3)) * 40 + ntile * 10) * 1024);
            const int kstart = (2 * (slot >> 2) + 4 * ntile + (ntile & 1)) & 15;
            if (wv < 4) dft_body<3>(lds0, wv, 0, rc, rws, csrc, absrc, kstart, ydst);
            else dft_body<2>(lds0, wv, 3, rc, rws, csrc, absrc, kstart, ydst);
            publish(ws, OFF_FG2 + ((mt * 4 + ntile) * 4 + kq) * 64, tag);
            if (threadIdx.x < 8) __hip_atomic_store((gu64*)(ws + OFF_FG3 + b * 64 + threadIdx.x * 8), tag, RLX_AGENT);
            STAMP(ws, 5);
        }
        if (PH & 4) {
            final_phase(xcd * 4 + (slot >> 3), slot & 7, wo, ws, out, lds0, wv, tag, bid);
            STAMP(ws, 9);
            if (b == 0 && wv == 0) {
                for (unsigned spins = 0; spins < (1u << 22); ++spins) {
                    bool ok = true;
#pragma unroll
                    for (int i = 0; i < 4; ++i)
                        ok = ok && __hip_atomic_load((gu64*)(ws + OFF_FA1 + (i * 64 + l) * 64), RLX_AGENT) == tag &&
                             __hip_atomic_load((gu64*)(ws + OFF_FG1 + (i * 64 + l) * 64), RLX_AGENT) == tag;
                    if (__all(ok)) break;
                    __builtin_amdgcn_s_sleep(2);
                }
                if (l < 8) __hip_atomic_store((gu64*)(ws + OFF_EPOCH + l * 8), tag, RLX_AGENT);
            }
        }
    }
}

extern "C" void kernel_launch(void* const* d_in, const int* in_sizes, int n_in, void* d_out, int out_size, void* d_ws,
                              size_t ws_size, hipStream_t stream) {
    const bf16* x = (const bf16*)d_in[0];
    const bf16* cr = (const bf16*)d_in[1];
    const bf16* ci = (const bf16*)d_in[2];
    const bf16* wr = (const bf16*)d_in[3];
    const bf16* wi = (const bf16*)d_in[4];
    const bf16* wqkv = (const bf16*)d_in[6];
    const bf16* wo = (const bf16*)d_in[7];
    const bf16* gw = (const bf16*)d_in[8];
    char* ws = (char*)d_ws;
    bf16* out = (bf16*)d_out;
    (void)hipFuncSetAttribute((const void*)fused_kernel<7>, hipFuncAttributeMaxDynamicSharedMemorySize, LDS_BYTES);
    fused_kernel<7><<<512, 512, LDS_BYTES, stream>>>(x, cr, ci, wr, wi, wqkv, wo, gw, ws, out);
}
```

```cpp
#include <hip/hip_runtime.h>
#include <hip/hip_bf16.h>
#include <stdint.h>
#define DIAGH 0
#define DIAGB 37

#define FUSED 1

typedef short bf16x8 __attribute__((ext_vector_type(8)));
typedef short s16x4 __attribute__((ext_vector_type(4)));
typedef float f32x16 __attribute__((ext_vector_type(16)));
typedef float f32x4 __attribute__((ext_vector_type(4)));
typedef float f32x2 __attribute__((ext_vector_type(2)));
typedef unsigned u32x4 __attribute__((ext_vector_type(4)));
typedef unsigned u32x2 __attribute__((ext_vector_type(2)));
typedef __bf16 bf16x2_t __attribute__((ext_vector_type(2)));
typedef unsigned long long u64;
typedef __attribute__((address_space(1))) u64 gu64;
typedef __hip_bfloat16 bf16;
#define LDSP __attribute__((address_space(3)))
#define DI __device__ __forceinline__
typedef __amdgpu_buffer_rsrc_t rsrc_t;

constexpr int S_ = 2048, D_ = 640, H_ = 32;
constexpr unsigned OFF_EPOCH = 0;
constexpr unsigned OFF_FA1 = 16384, OFF_FG1 = 32768, OFF_FG2 = 49152, OFF_FG3 = 65536, OFF_FA2 = 131072;
constexpr unsigned OFF_G = 262144;
constexpr unsigned OFF_Q1 = 524288;
constexpr unsigned OFF_Q2 = OFF_Q1 + H_ * 65536;
constexpr unsigned OFF_K2 = OFF_Q2 + 16 * 32768;
constexpr unsigned KV_CHUNK = 18432, KV_HEAD = 8 * KV_CHUNK;
constexpr unsigned OFF_KV = OFF_K2 + 16 * 32768;
constexpr unsigned OFF_AB = OFF_KV + H_ * KV_HEAD;
constexpr unsigned OFF_Y = OFF_AB + 4 * 64 * 20480;
constexpr unsigned OFF_OM = OFF_Y + 4 * S_ * D_ * 2;
constexpr unsigned OFF_OL = OFF_OM + S_ * 512 * 2;
constexpr unsigned WS_BYTES = OFF_OL + H_ * S_ * 8;
constexpr int LDS_BYTES = 81920;
constexpr unsigned SPIN_CAP = 1u << 16;

#define STAMP(ws, i) do { if (bid == DIAGB && threadIdx.x == 0) { volatile u64* p_ = (volatile u64*)((char*)(ws) + (128u << 20)); \
    if ((i) == 0) p_[0] = __builtin_amdgcn_s_memrealtime(); if ((i) == DIAGH) p_[1] = __builtin_amdgcn_s_memrealtime(); \
    if ((i) == 9) { const u64 a_ = p_[0], b_ = p_[1]; while (__builtin_amdgcn_s_memrealtime() - a_ < 10000 + 4 * (b_ - a_)) __builtin_amdgcn_s_sleep(4); } } } while (0)
#define TDECL
#define TREC(i) do { } while (0)
#define TDUMP(ws, n) do { } while (0)
#define MFMA32(a, b, c) __builtin_amdgcn_mfma_f32_32x32x16_bf16((a), (b), (c), 0, 0, 0)
#define MFMA16(a, b, c) __builtin_amdgcn_mfma_f32_16x16x32_bf16((a), (b), (c), 0, 0, 0)
#define WAIT_VM(n) asm volatile("s_waitcnt vmcnt(" #n ")" ::: "memory")
#define WAIT_VM_LGKM0(n) asm volatile("s_waitcnt vmcnt(" #n ") lgkmcnt(0)" ::: "memory")
#define BAR() asm volatile("s_barrier" ::: "memory")
#define EX2(v) __builtin_amdgcn_exp2f(v)
#define PIN(v) asm volatile("" : "+v"(v))
#define SB() __builtin_amdgcn_sched_barrier(0)
#define RLX_AGENT __ATOMIC_RELAXED, __HIP_MEMORY_SCOPE_AGENT

DI unsigned pk(float a, float b) { f32x2 v = {a, b}; return __builtin_bit_cast(unsigned, __builtin_convertvector(v, bf16x2_t)); }
DI float bf_lo(unsigned u) { return __uint_as_float(u << 16); }
DI float bf_hi(unsigned u) { return __uint_as_float(u & 0xffff0000u); }
DI rsrc_t mk_rsrc(const void* p, unsigned bytes) { return __builtin_amdgcn_make_buffer_rsrc((void*)p, (short)0, (int)bytes, 0x00020000); }
DI void dma16(unsigned lds_addr, unsigned voff, rsrc_t rs, unsigned soff) {
    asm volatile("s_mov_b32 m0, %0\n\ts_nop 0\n\tbuffer_load_dwordx4 %1, %2, %3 offen lds" ::"s"(lds_addr), "v"(voff), "s"(rs), "s"(soff) : "memory");
}
DI void dma16c(unsigned lds_addr, unsigned voff, rsrc_t rs, unsigned soff) {
    asm volatile("s_mov_b32 m0, %0\n\ts_nop 0\n\tbuffer_load_dwordx4 %1, %2, %3 offen sc1 lds" ::"s"(lds_addr), "v"(voff), "s"(rs), "s"(soff) : "memory");
}
DI void touch(unsigned lds_addr, unsigned voff, rsrc_t rs) {
    asm volatile("s_mov_b32 m0, %0\n\ts_nop 0\n\tbuffer_load_dword %1, %2, 0 offen lds" ::"s"(lds_addr), "v"(voff), "s"(rs) : "memory");
}
DI bf16x8 lds_rd16(unsigned a) { return *(const LDSP bf16x8*)(a); }
DI s16x4 lds_tr(unsigned a) { return __builtin_amdgcn_ds_read_tr16_b64_v4i16((LDSP s16x4*)(a)); }
DI bf16x8 lds_tr2(unsigned a0, unsigned a1) { return __builtin_shufflevector(lds_tr(a0), lds_tr(a1), 0, 1, 2, 3, 4, 5, 6, 7); }
DI void st16(rsrc_t rs, unsigned off, u32x4 v) { __builtin_amdgcn_raw_buffer_store_b128(v, rs, off, 0, 16); }
DI void st8(rsrc_t rs, unsigned off, u32x2 v) { __builtin_amdgcn_raw_buffer_store_b64(v, rs, off, 0, 16); }
DI void st4(rsrc_t rs, unsigned off, unsigned v) { __builtin_amdgcn_raw_buffer_store_b32(v, rs, off, 0, 16); }
DI u32x4 ld16(rsrc_t rs, unsigned off) { return __builtin_amdgcn_raw_buffer_load_b128(rs, off, 0, 16); }
DI int swap23(int i) { return (i & ~12) | ((i & 4) << 1) | ((i & 8) >> 1); }
DI u32x4 pack8(const f32x4& a, const f32x4& b) { return u32x4{pk(a[0], a[1]), pk(a[2], a[3]), pk(b[0], b[1]), pk(b[2], b[3])}; }

#define POLL_SLEEP 4
#define G_DELAY 9
#define A_DELAY 0
#define G_PRIO 2
#define DFT_PRIO 0
#define A_PRIO 3
#define ATT_PRIO 0
#define CODE_PF 39936
#define WO_PF 1
#define G_DELAY0 0
#define ROT 1
#define WARM 2
#define DFT_HI 10
#define SWAP_ROLES 0
DI u64 epoch_load(char* ws) {
    return __hip_atomic_load((gu64*)(ws + OFF_EPOCH), RLX_AGENT);
}
DI u64 epoch_tag(u64 e) {
    return (((u64)(unsigned)__builtin_amdgcn_readfirstlane((unsigned)(e >> 32)) << 32) | (unsigned)__builtin_amdgcn_readfirstlane((unsigned)e)) + 1;
}
DI void publish(char* ws, unsigned flag_off, u64 tag) {
    WAIT_VM(0);
    BAR();
    if (threadIdx.x < 8) __hip_atomic_store((gu64*)(ws + flag_off + threadIdx.x * 8), tag, RLX_AGENT);
}
DI void publish_wave(char* ws, unsigned flag_off, u64 tag) {
    WAIT_VM(0);
    if ((threadIdx.x & 63) < 8) __hip_atomic_store((gu64*)(ws + flag_off + (threadIdx.x & 63) * 8), tag, RLX_AGENT);
}
DI void await_wave(char* ws, unsigned off, u64 tag) {
    _Pragma("unroll 1") for (unsigned spins = 0; spins < SPIN_CAP; ++spins) {
        if (__all(__hip_atomic_load((gu64*)(ws + off), RLX_AGENT) == tag)) break;
        __builtin_amdgcn_s_sleep(POLL_SLEEP);
    }
    asm volatile("" ::: "memory");
}
DI void poll2(char* ws, unsigned off0, unsigned off1, u64 tag) {
    _Pragma("unroll 1") for (unsigned spins = 0; spins < SPIN_CAP; ++spins) {
        const u64 a = __hip_atomic_load((gu64*)(ws + off0), RLX_AGENT), b = __hip_atomic_load((gu64*)(ws + off1), RLX_AGENT);
        if (__all(a == tag && b == tag)) break;
        __builtin_amdgcn_s_sleep(POLL_SLEEP);
    }
}
DI void await2(char* ws, unsigned off0, unsigned off1, u64 tag, int wv) {
    if (wv == 0) poll2(ws, off0, off1, tag);
    asm volatile("s_waitcnt lgkmcnt(0)\n\ts_barrier" ::: "memory");
}

template <int KIND>
DI u64 qkv_body(const bf16* x, const bf16* wqkv, int m0, int nt, int k0, char* ws, unsigned lds0, int wv, int bid) {
    const int l = threadIdx.x & 63, wn = wv & 3, wm = wv >> 2, i16 = l & 15, g = l >> 4;
    const rsrc_t rx = mk_rsrc(x, S_ * D_ * 2), rw = mk_rsrc(wqkv, D_ * 1920 * 2);
    unsigned xoff, woff;
    {
        const int rho = 8 * wv + (l >> 3), i = rho & 15;
        const int s = KIND == 2 ? (rho & ~31) + 8 * (i >> 2) + 4 * ((rho >> 4) & 1) + (i & 3) : rho;
        xoff = (unsigned)((m0 + s) * (D_ * 2) + (((l & 7) ^ (l >> 3)) << 4));
        const int k = 4 * wv + (l >> 4), f = (k & 3) | (((k >> 3) & 1) << 2);
        woff = (unsigned)(k * 3840 + nt * 256 + (((i16 >> 1) ^ f) << 5) + ((i16 & 1) << 4));
    }
    auto piece = [&](int u, int i) {
        const unsigned dst = lds0 + (u % 5) * 16384 + i * 8192 + wv * 1024;
        const int ks = k0 + (u >> 1) - (k0 + (u >> 1) >= 10 ? 10 : 0);
        if (u & 1) dma16(dst, woff, rw, (ks * 64 + i * 32) * 3840);
        else dma16(dst, xoff, rx, ks * 128 + i * 64 * (D_ * 2));
    };
    TDECL; TREC(0);
#pragma unroll
    for (int u = 0; u < 5; ++u) { piece(u, 0); piece(u, 1); if (u == 1) BAR(); }
    const unsigned xrd = lds0 + (wm * 64 + i16) * 128 + ((g ^ (i16 & 7)) << 4);
    const unsigned wrd = lds0 + (8 * g + (i16 >> 2)) * 256 + (((2 * wn) ^ ((i16 >> 2) | ((g & 1) << 2))) << 5) + 8 * (l & 3);
    f32x4 acc[4][2];
#pragma unroll
    for (int m = 0; m < 4; ++m) { acc[m][0] = f32x4{0.f, 0.f, 0.f, 0.f}; acc[m][1] = f32x4{0.f, 0.f, 0.f, 0.f}; }
    bf16x8 xf[2][4], wf[2][2];
    u64 ep;
    auto rd1 = [&](int kt, int kk, int i) {
        const unsigned xs = ((2 * kt) % 5) * 16384, wsl = ((2 * kt + 1) % 5) * 16384;
        if (i < 2) { const unsigned a = (wrd ^ (i << 5)) + wsl + kk * 8192; wf[kk][i] = lds_tr2(a, a + 1024); }
        else if (i < 6) xf[kk][i - 2] = lds_rd16((xrd ^ (kk << 6)) + xs + (i - 2) * 2048);
    };
    auto mm = [&](int kk, int m) {
#pragma unroll
        for (int j = 0; j < 2; ++j) acc[m][j] = (KIND == 1) ? MFMA16(wf[kk][j], xf[kk][m], acc[m][j]) : MFMA16(xf[kk][m], wf[kk][j], acc[m][j]);
    };
#pragma unroll
    for (int kt = 0; kt < 10; ++kt) {
        TREC(3 * kt + 1);
        if (kt == 0) WAIT_VM_LGKM0(6); else if (kt < 9) WAIT_VM_LGKM0(2); else WAIT_VM_LGKM0(0);
        TREC(3 * kt + 2);
        BAR();
        TREC(3 * kt + 3);
        if (kt == 4) ep = epoch_load(ws);
        SB();
#pragma unroll
        for (int m = 0; m < 4; ++m) {
            if (kt > 0) mm(1, m);
            SB();
            rd1(kt, 0, 2 * m); rd1(kt, 0, 2 * m + 1);
            if (kt > 0 && 2 * kt + 3 + (m >> 1) < 20) piece(2 * kt + 3 + (m >> 1), m & 1);
            SB();
        }
#pragma unroll
        for (int m = 0; m < 4; ++m) {
            mm(0, m);
            SB();
            rd1(kt, 1, 2 * m); rd1(kt, 1, 2 * m + 1);
            SB();
        }
    }
#pragma unroll
    for (int m = 0; m < 4; ++m) mm(1, m);
    TREC(31); TDUMP(ws, 32);
    STAMP(ws, 1);
    const rsrc_t rws = mk_rsrc(ws, WS_BYTES);
    if (KIND == 2) {
#pragma unroll
        for (int j = 0; j < 2; ++j) {
            const int col = (nt - 10) * 128 + wn * 32 + 16 * j + i16;
            const int h = col / 20, d = col - h * 20;
            const int vp = d < 8 ? d : (d < 18 ? d - 8 : d - 10), vh = (d >= 4 && d < 12) || d == 16 || d == 17;
            const unsigned base = OFF_KV + (unsigned)(h * KV_HEAD + 8192 + vp * 1024 + vh * 512);
#pragma unroll
            for (int u = 0; u < 2; ++u) {
                const int s = m0 + wm * 64 + 32 * u + 8 * g;
                st16(rws, base + (s >> 8) * KV_CHUNK + (s & 255) * 2, pack8(acc[2 * u][j], acc[2 * u + 1][j]));
            }
        }
    } else {
        const bool isq = nt < 5;
        const float sc = isq ? 0.22360679774997896f * 1.4426950408889634f : 1.f;
        const int pos = isq ? (l & 31) : swap23(l & 31);
#pragma unroll
        for (int j = 0; j < 2; ++j) {
            const int c = (isq ? nt : nt - 5) * 128 + wn * 32 + 16 * j + 8 * (l >> 5);
            const int pair = c / 40, oc = (c - pair * 40) >> 3;
            const int h = 2 * pair + (oc > 2), half = oc > 2 ? oc - 3 : oc;
#pragma unroll
            for (int u = 0; u < 2; ++u) {
                const f32x4 a = acc[2 * u][j] * sc, b = acc[2 * u + 1][j] * sc;
                const auto r0 = __builtin_amdgcn_permlane16_swap(pk(a[0], a[1]), pk(b[0], b[1]), false, false);
                const auto r1 = __builtin_amdgcn_permlane16_swap(pk(a[2], a[3]), pk(b[2], b[3]), false, false);
                const int tile = (m0 + wm * 64 + 32 * u) >> 5;
                unsigned off;
                if (oc == 2) off = (isq ? OFF_Q2 : OFF_K2) + pair * 32768 + (tile * 32 + pos) * 16;
                else if (isq) off = OFF_Q1 + h * 65536 + tile * 1024 + (half * 32 + pos) * 16;
                else off = OFF_KV + h * KV_HEAD + (tile >> 3) * KV_CHUNK + (tile & 7) * 1024 + (half * 32 + pos) * 16;
                st16(rws, off, u32x4{r0[0], r1[0], r0[1], r1[1]});
            }
        }
    }
    WAIT_VM(0);
    return ep;
}

template <int WN>
DI u64 ab_body(const bf16* x, const bf16* w, bool isb, int m0, int n0, int k0, char* ws, unsigned lds0, int wv, int bid) {
    constexpr int NJ = WN == 0 ? 3 : 2, STG = 26624;
    const int l = threadIdx.x & 63, wm = wv & 3, i16 = l & 15, g = l >> 4;
    const rsrc_t rx = mk_rsrc(x, S_ * D_ * 2), rw = mk_rsrc(w, D_ * D_ * 2);
    unsigned xoff, woff;
    {
        const int rho = 8 * wv + (l >> 3), i = rho & 15;
        const int s = (rho & ~31) + 8 * (i >> 2) + 4 * ((rho >> 4) & 1) + (i & 3);
        xoff = (unsigned)((m0 + s) * (D_ * 2) + (((l & 7) ^ (l >> 3)) << 4));
        woff = (unsigned)((n0 + rho) * (D_ * 2) + (((l & 7) ^ (l >> 3)) << 4));
    }
    const bool extra = WN == 1 && wv < 6;
    auto piece = [&](int kt, int i) {
        const unsigned dst = lds0 + (kt % 3) * STG + wv * 1024;
        const int ks = k0 + kt - (k0 + kt >= 10 ? 10 : 0);
        if (i < 2) dma16(dst + i * 8192, xoff, rx, ks * 128 + i * 64 * (D_ * 2));
        else if (i == 2) dma16(dst + 16384, woff, rw, ks * 128);
        else if (WN == 1) { if (extra) dma16(dst + 16384 + 4096, woff, rw, ks * 128 + 32 * (D_ * 2)); }
    };
#pragma unroll
    for (int st = 0; st < 2; ++st)
#pragma unroll
        for (int i = 0; i < 4; ++i) piece(st, i);
    const unsigned xrd = lds0 + (wm * 32 + i16) * 128 + ((g ^ (i16 & 7)) << 4);
    const unsigned wrd = lds0 + 16384 + (WN * 48 + i16) * 128 + ((g ^ (i16 & 7)) << 4);
    f32x4 acc[2][NJ];
#pragma unroll
    for (int j = 0; j < NJ; ++j) { acc[0][j] = f32x4{0.f, 0.f, 0.f, 0.f}; acc[1][j] = f32x4{0.f, 0.f, 0.f, 0.f}; }
    bf16x8 xf[2][2], wf[2][NJ];
    u64 ep;
    auto rd1 = [&](int kt, int kk, int i) {
        const unsigned so = (kt % 3) * STG;
        if (i < 2) xf[kk][i] = lds_rd16((xrd ^ (kk << 6)) + so + i * 2048);
        else if (i - 2 < NJ) wf[kk][i - 2] = lds_rd16((wrd ^ (kk << 6)) + so + (i - 2) * 2048);
    };
    auto mm = [&](int kk, int j) {
#pragma unroll
        for (int m = 0; m < 2; ++m) acc[m][j] = MFMA16(xf[kk][m], wf[kk][j], acc[m][j]);
    };
#pragma unroll
    for (int kt = 0; kt < 10; ++kt) {
        if (kt < 9) { if (extra) WAIT_VM_LGKM0(4); else WAIT_VM_LGKM0(3); } else WAIT_VM_LGKM0(0);
        BAR();
        if (kt == 4) ep = epoch_load(ws);
        SB();
#pragma unroll
        for (int j = 0; j < NJ; ++j) {
            if (kt > 0) mm(1, j);
            SB();
            rd1(kt, 0, 2 * j); rd1(kt, 0, 2 * j + 1);
            if (kt + 2 < 10 && j < 2) piece(kt + 2, j);
            SB();
        }
#pragma unroll
        for (int j = 0; j < NJ; ++j) {
            mm(0, j);
            SB();
            rd1(kt, 1, 2 * j); rd1(kt, 1, 2 * j + 1);
            if (kt + 2 < 10 && j < 2) piece(kt + 2, 2 + j);
            SB();
        }
    }
#pragma unroll
    for (int j = 0; j < NJ; ++j) mm(1, j);
    STAMP(ws, 1);
    const rsrc_t rws = mk_rsrc(ws, WS_BYTES);
    const float sg = isb ? -1.f : 1.f;
    const int kg = (isb ? 2048 : 0) + m0 + 32 * wm + 8 * g;
#pragma unroll
    for (int j = 0; j < NJ; ++j) {
        const int e = n0 + WN * 48 + 16 * j + i16;
        const int ntile = e / 160, er = e - ntile * 160;
        st16(rws, OFF_AB + (unsigned)(ntile * 64 * 20480 + (er >> 5) * 1024 + swap23(er & 31) * 16 + (kg >> 4) * 5120 + ((kg >> 3) & 1) * 512),
             pack8(acc[0][j] * sg, acc[1][j] * sg));
    }
    WAIT_VM(0);
    return ep;
}

constexpr int AT_SLOT = 23728;

DI float half_swap_max(float m) {
    auto r = __builtin_amdgcn_permlane32_swap(__float_as_uint(m), __float_as_uint(m), false, false);
    return fmaxf(__uint_as_float(r[0]), __uint_as_float(r[1]));
}
DI float max16(const f32x16& s) {
    float a = fmaxf(fmaxf(s[0], s[1]), s[2]), b = fmaxf(fmaxf(s[3], s[4]), s[5]);
    a = fmaxf(fmaxf(a, s[6]), s[7]); b = fmaxf(fmaxf(b, s[8]), s[9]);
    a = fmaxf(fmaxf(a, s[10]), s[11]); b = fmaxf(fmaxf(b, s[12]), s[13]);
    a = fmaxf(fmaxf(a, s[14]), s[15]);
    return fmaxf(a, b);
}

DI void attn_phase(int head, int qb, char* ws, unsigned lds0, int wv, u64 tag, unsigned f0, unsigned f1, int bid) {
    const int l = threadIdx.x & 63, hh = l >> 5, r32 = l & 31;
    const int qt = qb * 8 + wv, pair = head >> 1, odd = head & 1;
    const rsrc_t rws = mk_rsrc(ws, WS_BYTES);
    const unsigned lane16 = l * 16;
    const unsigned kvsrc = OFF_KV + head * KV_HEAD + wv * 1024;
    const unsigned src3 = wv < 2 ? kvsrc + 16384 : OFF_K2 + pair * 32768 + (wv - 2) * 1024, step3 = wv < 2 ? KV_CHUNK : 4096;
    const unsigned dst3 = wv < 2 ? 12288 + (8 + wv) * 1040 : 8192 + (wv - 2) * 1024;
    auto load_chunk = [&](int c, int slot) {
        const unsigned dst = lds0 + slot * AT_SLOT;
        const int cc = (c + qb) & 7;
        dma16c(dst + wv * 1024, lane16, rws, kvsrc + cc * KV_CHUNK);
        dma16c(dst + 12288 + wv * 1040, lane16, rws, kvsrc + cc * KV_CHUNK + 8192);
        if (wv < 6) dma16c(dst + dst3, lane16, rws, src3 + cc * step3);
    };
    load_chunk(0, 0);
    const bf16x8 q1 = __builtin_bit_cast(bf16x8, ld16(rws, OFF_Q1 + head * 65536 + qt * 1024 + lane16));
    const u32x4 qr = ld16(rws, OFF_Q2 + pair * 32768 + (qt * 32 + r32) * 16);
    load_chunk(1, 1);
    if (wv < 3) {
        const unsigned c = hh ? 0u : 0x3F803F80u;
        *(LDSP u32x4*)(lds0 + wv * AT_SLOT + 12288 + 10400 + lane16 + hh * 16) = u32x4{c, c, c, c};
    }
    bf16x8 q2[2];
#pragma unroll
    for (int P = 0; P < 2; ++P) {
        const bool lo = hh == P && !odd, hi = hh == P && odd;
        q2[P] = __builtin_bit_cast(bf16x8, u32x4{lo ? qr[0] : 0u, lo ? qr[1] : 0u, hi ? qr[2] : 0u, hi ? qr[3] : 0u});
    }
    const bool vdata = r32 < 18 || r32 == 20 || r32 == 21, vone = r32 == 18 || r32 == 22;
    const int vp = r32 < 4 ? r32 : (r32 < 12 ? r32 - 4 : (r32 < 18 ? r32 - 8 : r32 - 12)), vh = (r32 >= 4 && r32 < 12) || r32 == 16 || r32 == 17;
    const unsigned voff = 12288 + hh * 16 + (vdata ? vp * 1040 + vh * 512 : (vone ? 10400 : 10928));
    const f32x16 zero16 = {0.f, 0.f, 0.f, 0.f, 0.f, 0.f, 0.f, 0.f, 0.f, 0.f, 0.f, 0.f, 0.f, 0.f, 0.f, 0.f};
    WAIT_VM_LGKM0(2);
    BAR();
    STAMP(ws, 4);
    unsigned kb = lds0 + lane16, vb = lds0 + voff;
    bf16x8 k2f = lds_rd16(kb + 8192);
    f32x16 sc = MFMA32(lds_rd16(kb), q1, zero16);
    sc = MFMA32(k2f, q2[0], sc);
    const float m0 = half_swap_max(max16(sc));
    f32x16 negm;
#pragma unroll
    for (int i = 0; i < 16; ++i) { negm[i] = -m0; sc[i] -= m0; }
    f32x16 o = zero16;
    bf16x8 k1f = lds_rd16(kb + 1024);
    bf16x8 v0f = lds_rd16(vb), v1f = lds_rd16(vb);
    u32x4 p0, p1 = {0u, 0u, 0u, 0u};
    float e12 = 0.f, e13 = 0.f, e14 = 0.f, e15 = 0.f;
    int slot = 0;
    for (int c = 0; c < 8; ++c) {
        const int nslot = slot == 2 ? 0 : slot + 1;
        const unsigned kbn = lds0 + nslot * AT_SLOT + lane16, vbn = lds0 + nslot * AT_SLOT + voff;
#pragma unroll
        for (int t = 0; t < 8; ++t) {
            if (t == 6) {
                if (c == 0 && wv == 0) poll2(ws, f0, f1, tag);
                WAIT_VM(0);
                BAR();
                if (c + 2 < 8) load_chunk(c + 2, nslot == 2 ? 0 : nslot + 1);
            }
            float a0, a1, a2, a3, b0, b1, b2, b3;
            SB();
            f32x16 sn = MFMA32(k1f, q1, negm);
            k1f = (t < 6) ? lds_rd16(kb + (t + 2) * 1024) : lds_rd16(kbn + (t - 6) * 1024);
            p1[2] = pk(e12, e13); p1[3] = pk(e14, e15); PIN(p1);
            a0 = EX2(sc[0]); a1 = EX2(sc[1]); a2 = EX2(sc[2]); a3 = EX2(sc[3]); PIN(a0); PIN(a1); PIN(a2); PIN(a3);
            SB();
            sn = MFMA32(k2f, q2[(t + 1) & 1], sn);
            if ((t & 1) == 0) k2f = (t < 6) ? lds_rd16(kb + 8192 + (t + 2) * 512) : lds_rd16(kbn + 8192);
            p0[0] = pk(a0, a1); p0[1] = pk(a2, a3);
            b0 = EX2(sc[4]); b1 = EX2(sc[5]); b2 = EX2(sc[6]); b3 = EX2(sc[7]); PIN(b0); PIN(b1); PIN(b2); PIN(b3);
            SB();
            o = MFMA32(v1f, __builtin_bit_cast(bf16x8, p1), o);
            v1f = lds_rd16(vb + t * 64 + 32);
            p0[2] = pk(b0, b1); p0[3] = pk(b2, b3); PIN(p0);
            a0 = EX2(sc[8]); a1 = EX2(sc[9]); a2 = EX2(sc[10]); a3 = EX2(sc[11]); PIN(a0); PIN(a1); PIN(a2); PIN(a3);
            SB();
            o = MFMA32(v0f, __builtin_bit_cast(bf16x8, p0), o);
            v0f = (t < 7) ? lds_rd16(vb + (t + 1) * 64) : lds_rd16(vbn);
            p1[0] = pk(a0, a1); p1[1] = pk(a2, a3);
            e12 = EX2(sc[12]); e13 = EX2(sc[13]); e14 = EX2(sc[14]); e15 = EX2(sc[15]); PIN(e12); PIN(e13); PIN(e14); PIN(e15);
            SB();
            sc = sn;
        }
        slot = nslot; kb = kbn; vb = vbn;
    }
    p1[2] = pk(e12, e13); p1[3] = pk(e14, e15);
    o = MFMA32(v1f, __builtin_bit_cast(bf16x8, p1), o);
    STAMP(ws, 5);
    float asum = 0.f;
#pragma unroll
    for (int i = 0; i < 10; ++i) asum += fabsf(o[i]);
    const bool bad = !(o[10] <= 1.0e18f) || !(asum <= 1.0e37f);
    if (__builtin_expect(__any(bad), 0)) {
        const unsigned vg = 8192 + hh * 16 + (vdata ? vp * 1024 + vh * 512 : 0), vc = vone ? 0x3F803F80u : 0u;
        const u32x4 vconst = {vc, vc, vc, vc};
        float m = -INFINITY;
        o = zero16;
        for (int t = 0; t < 64; ++t) {
            const unsigned cb = OFF_KV + head * KV_HEAD + (t >> 3) * KV_CHUNK;
            const int T = t & 7;
            f32x16 s = MFMA32(__builtin_bit_cast(bf16x8, ld16(rws, cb + T * 1024 + lane16)), q1, zero16);
            s = MFMA32(__builtin_bit_cast(bf16x8, ld16(rws, OFF_K2 + pair * 32768 + (t * 32 + r32) * 16)), q2[0], s);
            const float mn = fmaxf(m, half_swap_max(max16(s)));
            const float al = EX2(m - mn);
            m = mn;
#pragma unroll
            for (int i = 0; i < 16; ++i) { o[i] *= al; s[i] = EX2(s[i] - mn); }
            const u32x4 pa = {pk(s[0], s[1]), pk(s[2], s[3]), pk(s[4], s[5]), pk(s[6], s[7])};
            const u32x4 pb = {pk(s[8], s[9]), pk(s[10], s[11]), pk(s[12], s[13]), pk(s[14], s[15])};
            const u32x4 va = ld16(rws, cb + vg + T * 64), vb2 = ld16(rws, cb + vg + T * 64 + 32);
            o = MFMA32(__builtin_bit_cast(bf16x8, vdata ? va : vconst), __builtin_bit_cast(bf16x8, pa), o);
            o = MFMA32(__builtin_bit_cast(bf16x8, vdata ? vb2 : vconst), __builtin_bit_cast(bf16x8, pb), o);
        }
    }
    const float rl = 1.f / o[10];
    const int q = qt * 32 + r32;
    st16(rws, OFF_OM + ((q >> 4) * 16 + pair) * 1024 + (odd * 2 + hh) * 256 + (q & 15) * 16,
         u32x4{pk(o[0] * rl, o[1] * rl), pk(o[2] * rl, o[3] * rl), pk(o[4] * rl, o[5] * rl), pk(o[6] * rl, o[7] * rl)});
    const unsigned xl = pk(o[8] * rl, o[9] * rl);
    const auto rx = __builtin_amdgcn_permlane32_swap(xl, xl, false, false);
    if (!hh) st8(rws, OFF_OL + (head * S_ + q) * 8, u32x2{rx[0], rx[1]});
}

template <int NE>
DI void dft_body(unsigned lds0, int wv, int e0, rsrc_t rc, rsrc_t rws, unsigned csrc, unsigned absrc, int kstart, unsigned ydst) {
    const int l = threadIdx.x & 63, hh = l >> 5, r32 = l & 31, wm = wv & 3;
    const unsigned lane16 = l * 16;
    const unsigned coff = (unsigned)((8 * wv + (l >> 3)) * 4096 + (((l & 7) ^ ((4 * wv + (l >> 4)) & 7)) << 4));
    auto piece = [&](int i, int slot, int ks) {
        const unsigned base = lds0 + slot * 36864 + wv * 1024;
        const unsigned as = absrc + ks * 20480 + wv * 1024;
        if (i == 0) dma16(base, coff, rc, csrc + ks * 128);
        else if (i == 1) dma16(base + 8192, coff, rc, csrc + ks * 128 + 64 * 4096);
        else if (i == 2) dma16c(base + 16384, lane16, rws, as);
        else if (i == 3) dma16c(base + 16384 + 8192, lane16, rws, as + 8192);
        else if (NE == 2) dma16c(base + 16384 + 12288, lane16, rws, as + 12288);
    };
    int ksn = kstart;
#pragma unroll
    for (int i = 0; i < 5; ++i) piece(i, 0, ksn);
    const int row = wm * 32 + r32;
    const unsigned crd = lds0 + row * 128 + ((hh ^ ((row >> 1) & 7)) << 4);
    const unsigned ard = lds0 + 16384 + e0 * 1024 + lane16;
    f32x16 acc[NE];
#pragma unroll
    for (int e = 0; e < NE; ++e)
#pragma unroll
        for (int i = 0; i < 16; ++i) acc[e][i] = 0.f;
    bf16x8 cf[2], af[2][NE];
    const bf16x8 z8 = {0, 0, 0, 0, 0, 0, 0, 0};
    cf[1] = z8;
#pragma unroll
    for (int e = 0; e < NE; ++e) af[1][e] = z8;
    auto quarter = [&](int kk, unsigned so, bool pre, int slot) {
#pragma unroll
        for (int e = 0; e < NE; ++e) {
            acc[e] = MFMA32(af[(kk + 1) & 1][e], cf[(kk + 1) & 1], acc[e]);
            SB();
            if (e == 0) cf[kk & 1] = lds_rd16((crd ^ (kk << 5)) + so);
            af[kk & 1][e] = lds_rd16(ard + so + (kk * 5 + e) * 1024);
            if (pre && e < 2 && kk * 2 + e < 5) piece(kk * 2 + e, slot, ksn);
            SB();
        }
    };
#pragma unroll 2
    for (int ks = 0; ks < 16; ++ks) {
        WAIT_VM_LGKM0(0);
        BAR();
        if (ks == DFT_HI) __builtin_amdgcn_s_setprio(DFT_PRIO);
        ksn = (ksn + 1) & 15;
        const unsigned so = (ks & 1) * 36864;
        const bool pre = ks + 1 < 16;
        SB();
#pragma unroll
        for (int kk = 0; kk < 4; ++kk) quarter(kk, so, pre, (ks + 1) & 1);
    }
    __builtin_amdgcn_s_setprio(G_PRIO);
#pragma unroll
    for (int e = 0; e < NE; ++e) acc[e] = MFMA32(af[1][e], cf[1], acc[e]);
#pragma unroll
    for (int e = 0; e < NE; ++e)
#pragma unroll
        for (int s = 0; s < 2; ++s) {
            const u32x4 v = {pk(acc[e][8 * s], acc[e][8 * s + 1]), pk(acc[e][8 * s + 2], acc[e][8 * s + 3]),
                             pk(acc[e][8 * s + 4], acc[e][8 * s + 5]), pk(acc[e][8 * s + 6], acc[e][8 * s + 7])};
            st16(rws, ydst + ((e0 + e) * 2 + s) * 1024 + lane16, v);
        }
}

DI void final_phase(int mt, int nt, const bf16* wo, char* ws, bf16* out, unsigned lds0, int wv, u64 tag, int bid) {
    const int l = threadIdx.x & 63, kq = wv & 3, th = wv >> 2, i16 = l & 15, g = l >> 4;
    const rsrc_t rwo = mk_rsrc(wo, D_ * D_ * 2), rws = mk_rsrc(ws, WS_BYTES);
    const unsigned wsrc = (unsigned)(kq * 160 * (D_ * 2) + nt * 160);
    const unsigned wrd = lds0 + kq * 20480 + ((g >> 1) * 16 + (g & 1) * 4 + (i16 >> 2)) * 160 + 8 * (l & 3);
#pragma unroll
    for (int i = 0; i < 3; ++i) {
        const int c = (th * 3 + i) * 64 + l, rho = c / 10, ka = swap23(rho);
        if (th == 0 || i < 2) dma16(lds0 + kq * 20480 + (th * 3 + i) * 1024, (unsigned)((20 * (ka >> 2) + 16 + (ka & 3)) * (D_ * 2) + (c - rho * 10) * 16), rwo, wsrc);
    }
    unsigned woff[5];
#pragma unroll
    for (int i = 0; i < 5; ++i) {
        const int c = i * 64 + l, rho = c / 10, ka = swap23(rho);
        woff[i] = (unsigned)((20 * (ka >> 4) + (ka & 15)) * (D_ * 2) + (c - rho * 10) * 16);
    }
    WAIT_VM(0);
    BAR();
    bf16x8 w4[5];
#pragma unroll
    for (int e = 0; e < 5; ++e) w4[e] = lds_tr2(wrd + e * 32, wrd + e * 32 + 1280);
    asm volatile("s_waitcnt lgkmcnt(0)\n\ts_barrier" ::: "memory");
#pragma unroll
    for (int b = 0; b < 2; ++b)
#pragma unroll
        for (int i = 0; i < 5; ++i) dma16(lds0 + kq * 20480 + (th * 2 + b) * 5120 + i * 1024, woff[i], rwo, wsrc + (th * 2 + b) * 40 * (D_ * 2));
    await2(ws, OFF_FG2 + (((mt >> 1) * 4 + (nt >> 1)) * 4 + (l & 3)) * 64, OFF_FG3 + (mt * 8 + (l & 7)) * 64, tag, wv);
    STAMP(ws, 6);
    u32x4 yq[2]; float gv[2];
#pragma unroll
    for (int it = 0; it < 2; ++it) {
        if (it == 1 && wv >= 2) break;
        const int item = wv + 8 * it, t32 = item / 5, e16 = item - t32 * 5;
        u32x4 yv[4];
#pragma unroll
        for (int p = 0; p < 4; ++p) yv[p] = ld16(rws, OFF_Y + (unsigned)(((p * 64 + mt * 2 + t32) * 40 + nt * 5 + e16) * 1024 + l * 16));
        gv[it] = __uint_as_float(__builtin_amdgcn_raw_buffer_load_b32(rws, OFF_G + (mt * 64 + t32 * 32 + (l & 31)) * 64, 0, 16));
#pragma unroll
        for (int j = 0; j < 4; ++j)
            yq[it][j] = pk((bf_lo(yv[0][j]) + bf_lo(yv[1][j])) + (bf_lo(yv[2][j]) + bf_lo(yv[3][j])),
                           (bf_hi(yv[0][j]) + bf_hi(yv[1][j])) + (bf_hi(yv[2][j]) + bf_hi(yv[3][j])));
        PIN(yq[it]);
    }
    WAIT_VM(0);
    STAMP(ws, 7);
    BAR();
    await_wave(ws, OFF_FA2 + ((mt * 2 + th) * 32 + kq * 8 + (l & 7)) * 64, tag);
    STAMP(ws, 8);
    bf16x8 of[5][2];
#pragma unroll
    for (int k = 0; k < 4; ++k)
#pragma unroll
        for (int t = 0; t < 2; ++t)
            of[k][t] = __builtin_bit_cast(bf16x8, ld16(rws, OFF_OM + (unsigned)(((mt * 4 + th * 2 + t) * 16 + kq * 4 + k) * 1024 + l * 16)));
#pragma unroll
    for (int t = 0; t < 2; ++t) {
        const unsigned off = OFF_OL + (unsigned)(((kq * 8 + 2 * g) * S_ + mt * 64 + th * 32 + t * 16 + i16) * 8);
        const u32x2 lo = __builtin_amdgcn_raw_buffer_load_b64(rws, off, 0, 16), hi = __builtin_amdgcn_raw_buffer_load_b64(rws, off + S_ * 8, 0, 16);
        of[4][t] = __builtin_bit_cast(bf16x8, u32x4{lo[0], lo[1], hi[0], hi[1]});
    }
    SB();
    f32x4 acc[5][2];
#pragma unroll
    for (int e = 0; e < 5; ++e) { acc[e][0] = f32x4{0.f, 0.f, 0.f, 0.f}; acc[e][1] = f32x4{0.f, 0.f, 0.f, 0.f}; }
#pragma unroll
    for (int k = 0; k < 5; ++k)
#pragma unroll
        for (int e = 0; e < 5; ++e) {
            const bf16x8 w = k < 4 ? lds_tr2(wrd + k * 5120 + e * 32, wrd + k * 5120 + e * 32 + 1280) : w4[e];
#pragma unroll
            for (int t = 0; t < 2; ++t) acc[e][t] = MFMA16(w, of[k][t], acc[e][t]);
        }
    asm volatile("s_waitcnt lgkmcnt(0)\n\ts_barrier" ::: "memory");
#pragma unroll
    for (int e = 0; e < 5; ++e)
#pragma unroll
        for (int t = 0; t < 2; ++t)
            *(LDSP f32x4*)(lds0 + (kq * 64 + th * 32 + t * 16 + i16) * 320 + e * 64 + ((g ^ (i16 >> 2)) << 4)) = acc[e][t];
    asm volatile("s_waitcnt lgkmcnt(0)\n\ts_barrier" ::: "memory");
#pragma unroll
    for (int it = 0; it < 2; ++it) {
        if (it == 1 && wv >= 2) break;
        const int item = wv + 8 * it, t32 = item / 5, e16 = item - t32 * 5;
        const int tl = t32 * 32 + (l & 31), sw = (l >> 2) & 3, c0 = (l >> 5) * 2;
        f32x4 s0 = {0.f, 0.f, 0.f, 0.f}, s1 = {0.f, 0.f, 0.f, 0.f};
#pragma unroll
        for (int p = 0; p < 4; ++p) {
            s0 += *(const LDSP f32x4*)(lds0 + (p * 64 + tl) * 320 + e16 * 64 + ((c0 ^ sw) << 4));
            s1 += *(const LDSP f32x4*)(lds0 + (p * 64 + tl) * 320 + e16 * 64 + (((c0 + 1) ^ sw) << 4));
        }
        const float gg = gv[it], g1 = 1.f - gg;
        const u32x4 y = yq[it];
        const u32x4 v = {pk(gg * bf_lo(y[0]) + g1 * s0[0], gg * bf_hi(y[0]) + g1 * s0[1]), pk(gg * bf_lo(y[1]) + g1 * s0[2], gg * bf_hi(y[1]) + g1 * s0[3]),
                         pk(gg * bf_lo(y[2]) + g1 * s1[0], gg * bf_hi(y[2]) + g1 * s1[1]), pk(gg * bf_lo(y[3]) + g1 * s1[2], gg * bf_hi(y[3]) + g1 * s1[3])};
        *(u32x4*)((char*)out + (unsigned)((mt * 64 + tl) * (D_ * 2) + (nt * 80 + e16 * 16 + (l >> 5) * 8) * 2)) = v;
    }
}

template <int PH>
__global__ __launch_bounds__(512, 4) void fused_kernel(const bf16* x, const bf16* cr, const bf16* ci, const bf16* wr, const bf16* wi,
                                                       const bf16* wqkv, const bf16* wo, const bf16* gw, char* ws, bf16* out) {
    extern __shared__ __attribute__((aligned(16))) char smem[];
    const unsigned lds0 = (unsigned)(uintptr_t)(LDSP char*)smem;
    const int bid = blockIdx.x ^ (SWAP_ROLES << 8), b = bid & 255, xcd = b & 7, slot = b >> 3, l = threadIdx.x & 63;
    const int wv = __builtin_amdgcn_readfirstlane(threadIdx.x >> 6);
    u64 tag = 0;
    const int mt1 = (xcd & 1) * 8 + (slot & 7), nt1 = (xcd >> 1) * 4 + (slot >> 3);
    const int k0 = ROT ? (2 * (slot & 7) + (((slot >> 3) * 5 + 1) >> 1)) % 10 : 0;
    const char* pc;
    asm volatile("s_getpc_b64 %0" : "=s"(pc));
    if (PH & 1) STAMP(ws, 0);
    if (__builtin_expect(bid < 256, 1)) {
        if (PH & 1) {
            __builtin_amdgcn_s_setprio(A_PRIO);
            for (int i = 0; i < A_DELAY; ++i) __builtin_amdgcn_s_sleep(16);
            u64 ep;
            if (nt1 < 10) ep = qkv_body<1>(x, wqkv, mt1 * 128, nt1, k0, ws, lds0, wv, bid);
            else if (nt1 < 15) ep = qkv_body<2>(x, wqkv, mt1 * 128, nt1, k0, ws, lds0, wv, bid);
            else ep = epoch_load(ws);
            tag = epoch_tag(ep);
            publish(ws, OFF_FA1 + (nt1 * 16 + mt1) * 64, tag);
            STAMP(ws, 2);
        }
        if (PH & 2) {
            const int head = xcd * 4 + (slot >> 3), qb = slot & 7;
            const int n0 = ((head >> 1) * 40) >> 7, n1 = ((head >> 1) * 40 + 39) >> 7;
            const int sec = (l >> 2) & 3, sb = sec == 3 ? 0 : sec * 5;
            const int mrow = (qb * 2 + (sec == 1 || sec == 2 ? (l & 3) : (l & 1))) & 15;
            await2(ws, OFF_FA1 + ((sb + n0) * 16 + mrow) * 64, OFF_FA1 + ((sb + n1) * 16 + mrow) * 64, tag, wv);
            STAMP(ws, 3);
            __builtin_amdgcn_s_setprio(ATT_PRIO);
            const int sb2 = 5 + ((l >> 4) & 1) * 5;
            attn_phase(head, qb, ws, lds0, wv, tag, OFF_FA1 + ((sb2 + n0) * 16 + (l & 15)) * 64, OFF_FA1 + ((sb2 + n1) * 16 + (l & 15)) * 64, bid);
            publish_wave(ws, OFF_FA2 + ((qb * 8 + wv) * 32 + head) * 64, tag);
            STAMP(ws, 9);
        }
    } else {
        const rsrc_t rws = mk_rsrc(ws, WS_BYTES);
        __builtin_amdgcn_s_setprio(G_PRIO);
        if (PH & 1) {
            {
                const unsigned td = lds0 + 79872 + wv * 256;
                if (wv == 0) touch(td, l < 10 ? (unsigned)((l * 32 + slot) * 128) : 0x7ffffff0u, mk_rsrc(pc, CODE_PF));
                const int id = slot * 8 + wv;
                const int dl = id * 13 + l;
                touch(td, l < 13 && dl < 3200 ? (unsigned)((((xcd >> 1) & 1) * 3200 + dl) * 128) : 0x7ffffff0u, mk_rsrc(xcd >= 4 ? wi : wr, D_ * D_ * 2));
            }
            for (int i = 0; i < G_DELAY0; ++i) __builtin_amdgcn_s_sleep(16);
            if (wv == 0) touch(lds0 + 79872, l < 25 ? (unsigned)((b * 25 + l) * 128) : 0x7ffffff0u, mk_rsrc(wo, D_ * D_ * 2));
            for (int i = G_DELAY0; i < G_DELAY; ++i) __builtin_amdgcn_s_sleep(16);
            const bool isb = nt1 >= 8;
            u64 ep;
            if (wv < 4) ep = ab_body<0>(x, isb ? wi : wr, isb, mt1 * 128, (nt1 & 7) * 80, k0, ws, lds0, wv, bid);
            else ep = ab_body<1>(x, isb ? wi : wr, isb, mt1 * 128, (nt1 & 7) * 80, k0, ws, lds0, wv, bid);
            tag = epoch_tag(ep);
            publish(ws, OFF_FG1 + (nt1 * 16 + mt1) * 64, tag);
            STAMP(ws, 2);
            const int row = b * 8 + wv;
            const unsigned* xr = (const unsigned*)(x + row * D_);
            const unsigned* gp = (const unsigned*)gw;
            unsigned xa[5], ga[5];
#pragma unroll
            for (int i = 0; i < 5; ++i) { xa[i] = xr[l + 64 * i]; ga[i] = gp[l + 64 * i]; }
            float s = 0.f;
#pragma unroll
            for (int i = 0; i < 5; ++i) s += bf_lo(xa[i]) * bf_lo(ga[i]) + bf_hi(xa[i]) * bf_hi(ga[i]);
#pragma unroll
            for (int off = 32; off > 0; off >>= 1) s += __shfl_xor(s, off);
            if (l < 16) st4(rws, OFF_G + row * 64 + l * 4, __float_as_uint(1.f / (1.f + __expf(-s))));
        }
        if (PH & 2) {
            const int kq = xcd & 3, mt = (xcd >> 2) * 8 + (slot >> 2), ntile = slot & 3;
            const unsigned fo = OFF_FG1 + (((kq >> 1) * 8 + ntile * 2) * 16 + (kq & 1) * 8 + (l & 7)) * 64;
            await2(ws, fo, fo + 1024, tag, wv);
            STAMP(ws, 3);
            const rsrc_t rc = mk_rsrc(kq < 2 ? cr : ci, S_ * S_ * 2);
            const unsigned csrc = (unsigned)(mt * 128 * 4096 + (kq & 1) * 2048);
            const unsigned absrc = OFF_AB + (unsigned)((ntile * 64 + kq * 16) * 20480);
            const unsigned ydst = OFF_Y + (unsigned)(((kq * 64 + mt * 4 + (wv & 3)) * 40 + ntile * 10) * 1024);
            const int kstart = (2 * (slot >> 2) + 4 * ntile + (ntile & 1)) & 15;
            if (wv < 4) dft_body<3>(lds0, wv, 0, rc, rws, csrc, absrc, kstart, ydst);
            else dft_body<2>(lds0, wv, 3, rc, rws, csrc, absrc, kstart, ydst);
            publish(ws, OFF_FG2 + ((mt * 4 + ntile) * 4 + kq) * 64, tag);
            if (threadIdx.x < 8) __hip_atomic_store((gu64*)(ws + OFF_FG3 + b * 64 + threadIdx.x * 8), tag, RLX_AGENT);
            STAMP(ws, 5);
        }
        if (PH & 4) {
            final_phase(xcd * 4 + (slot >> 3), slot & 7, wo, ws, out, lds0, wv, tag, bid);
            STAMP(ws, 9);
            if (b == 0 && wv == 0) {
                for (unsigned spins = 0; spins < (1u << 22); ++spins) {
                    bool ok = true;
#pragma unroll
                    for (int i = 0; i < 4; ++i)
                        ok = ok && __hip_atomic_load((gu64*)(ws + OFF_FA1 + (i * 64 + l) * 64), RLX_AGENT) == tag &&
                             __hip_atomic_load((gu64*)(ws + OFF_FG1 + (i * 64 + l) * 64), RLX_AGENT) == tag;
                    if (__all(ok)) break;
                    __builtin_amdgcn_s_sleep(2);
                }
                if (l < 8) __hip_atomic_store((gu64*)(ws + OFF_EPOCH + l * 8), tag, RLX_AGENT);
            }
        }
    }
}

extern "C" void kernel_launch(void* const* d_in, const int* in_sizes, int n_in, void* d_out, int out_size, void* d_ws,
                              size_t ws_size, hipStream_t stream) {
    const bf16* x = (const bf16*)d_in[0];
    const bf16* cr = (const bf16*)d_in[1];
    const bf16* ci = (const bf16*)d_in[2];
    const bf16* wr = (const bf16*)d_in[3];
    const bf16* wi = (const bf16*)d_in[4];
    const bf16* wqkv = (const bf16*)d_in[6];
    const bf16* wo = (const bf16*)d_in[7];
    const bf16* gw = (const bf16*)d_in[8];
    char* ws = (char*)d_ws;
    bf16* out = (bf16*)d_out;
    (void)hipFuncSetAttribute((const void*)fused_kernel<7>, hipFuncAttributeMaxDynamicSharedMemorySize, LDS_BYTES);
    fused_kernel<7><<<512, 512, LDS_BYTES, stream>>>(x, cr, ci, wr, wi, wqkv, wo, gw, ws, out);
}
```

```cpp
#include <hip/hip_runtime.h>
#include <hip/hip_bf16.h>
#include <stdint.h>
#define SPB 0

#define FUSED 1

typedef short bf16x8 __attribute__((ext_vector_type(8)));
typedef short s16x4 __attribute__((ext_vector_type(4)));
typedef float f32x16 __attribute__((ext_vector_type(16)));
typedef float f32x4 __attribute__((ext_vector_type(4)));
typedef float f32x2 __attribute__((ext_vector_type(2)));
typedef unsigned u32x4 __attribute__((ext_vector_type(4)));
typedef unsigned u32x2 __attribute__((ext_vector_type(2)));
typedef __bf16 bf16x2_t __attribute__((ext_vector_type(2)));
typedef unsigned long long u64;
typedef __attribute__((address_space(1))) u64 gu64;
typedef __hip_bfloat16 bf16;
#define LDSP __attribute__((address_space(3)))
#define DI __device__ __forceinline__
typedef __amdgpu_buffer_rsrc_t rsrc_t;

constexpr int S_ = 2048, D_ = 640, H_ = 32;
constexpr unsigned OFF_EPOCH = 0;
constexpr unsigned OFF_FA1 = 16384, OFF_FG1 = 32768, OFF_FG2 = 49152, OFF_FG3 = 65536, OFF_FA2 = 131072;
constexpr unsigned OFF_G = 262144;
constexpr unsigned OFF_Q1 = 524288;
constexpr unsigned OFF_Q2 = OFF_Q1 + H_ * 65536;
constexpr unsigned OFF_K2 = OFF_Q2 + 16 * 32768;
constexpr unsigned KV_CHUNK = 18432, KV_HEAD = 8 * KV_CHUNK;
constexpr unsigned OFF_KV = OFF_K2 + 16 * 32768;
constexpr unsigned OFF_AB = OFF_KV + H_ * KV_HEAD;
constexpr unsigned OFF_Y = OFF_AB + 4 * 64 * 20480;
constexpr unsigned OFF_OM = OFF_Y + 4 * S_ * D_ * 2;
constexpr unsigned OFF_OL = OFF_OM + S_ * 512 * 2;
constexpr unsigned WS_BYTES = OFF_OL + H_ * S_ * 8;
constexpr int LDS_BYTES = 81920;
constexpr unsigned SPIN_CAP = 1u << 16;

#define STAMP(ws, i) do { } while (0)
#define TDECL
#define TREC(i) do { } while (0)
#define TDUMP(ws, n) do { } while (0)
#define MFMA32(a, b, c) __builtin_amdgcn_mfma_f32_32x32x16_bf16((a), (b), (c), 0, 0, 0)
#define MFMA16(a, b, c) __builtin_amdgcn_mfma_f32_16x16x32_bf16((a), (b), (c), 0, 0, 0)
#define WAIT_VM(n) asm volatile("s_waitcnt vmcnt(" #n ")" ::: "memory")
#define WAIT_VM_LGKM0(n) asm volatile("s_waitcnt vmcnt(" #n ") lgkmcnt(0)" ::: "memory")
#define BAR() asm volatile("s_barrier" ::: "memory")
#define EX2(v) __builtin_amdgcn_exp2f(v)
#define PIN(v) asm volatile("" : "+v"(v))
#define SB() __builtin_amdgcn_sched_barrier(0)
#define RLX_AGENT __ATOMIC_RELAXED, __HIP_MEMORY_SCOPE_AGENT

DI unsigned pk(float a, float b) { f32x2 v = {a, b}; return __builtin_bit_cast(unsigned, __builtin_convertvector(v, bf16x2_t)); }
DI float bf_lo(unsigned u) { return __uint_as_float(u << 16); }
DI float bf_hi(unsigned u) { return __uint_as_float(u & 0xffff0000u); }
DI rsrc_t mk_rsrc(const void* p, unsigned bytes) { return __builtin_amdgcn_make_buffer_rsrc((void*)p, (short)0, (int)bytes, 0x00020000); }
DI void dma16(unsigned lds_addr, unsigned voff, rsrc_t rs, unsigned soff) {
    asm volatile("s_mov_b32 m0, %0\n\ts_nop 0\n\tbuffer_load_dwordx4 %1, %2, %3 offen lds" ::"s"(lds_addr), "v"(voff), "s"(rs), "s"(soff) : "memory");
}
DI void dma16c(unsigned lds_addr, unsigned voff, rsrc_t rs, unsigned soff) {
    asm volatile("s_mov_b32 m0, %0\n\ts_nop 0\n\tbuffer_load_dwordx4 %1, %2, %3 offen sc1 lds" ::"s"(lds_addr), "v"(voff), "s"(rs), "s"(soff) : "memory");
}
DI void touch(unsigned lds_addr, unsigned voff, rsrc_t rs) {
    asm volatile("s_mov_b32 m0, %0\n\ts_nop 0\n\tbuffer_load_dword %1, %2, 0 offen lds" ::"s"(lds_addr), "v"(voff), "s"(rs) : "memory");
}
DI bf16x8 lds_rd16(unsigned a) { return *(const LDSP bf16x8*)(a); }
DI s16x4 lds_tr(unsigned a) { return __builtin_amdgcn_ds_read_tr16_b64_v4i16((LDSP s16x4*)(a)); }
DI bf16x8 lds_tr2(unsigned a0, unsigned a1) { return __builtin_shufflevector(lds_tr(a0), lds_tr(a1), 0, 1, 2, 3, 4, 5, 6, 7); }
DI void st16(rsrc_t rs, unsigned off, u32x4 v) { __builtin_amdgcn_raw_buffer_store_b128(v, rs, off, 0, 16); }
DI void st8(rsrc_t rs, unsigned off, u32x2 v) { __builtin_amdgcn_raw_buffer_store_b64(v, rs, off, 0, 16); }
DI void st4(rsrc_t rs, unsigned off, unsigned v) { __builtin_amdgcn_raw_buffer_store_b32(v, rs, off, 0, 16); }
DI u32x4 ld16(rsrc_t rs, unsigned off) { return __builtin_amdgcn_raw_buffer_load_b128(rs, off, 0, 16); }
DI int swap23(int i) { return (i & ~12) | ((i & 4) << 1) | ((i & 8) >> 1); }
DI u32x4 pack8(const f32x4& a, const f32x4& b) { return u32x4{pk(a[0], a[1]), pk(a[2], a[3]), pk(b[0], b[1]), pk(b[2], b[3])}; }

#define POLL_SLEEP 4
#define G_DELAY 9
#define A_DELAY 0
#define G_PRIO 2
#define DFT_PRIO 0
#define A_PRIO 3
#define ATT_PRIO 0
#define CODE_PF 39936
#define WO_PF 1
#define G_DELAY0 0
#define ROT 1
#define WARM 2
#define DFT_HI 10
#define SWAP_ROLES 0
DI u64 epoch_load(char* ws) {
    return __hip_atomic_load((gu64*)(ws + OFF_EPOCH), RLX_AGENT);
}
DI u64 epoch_tag(u64 e) {
    return (((u64)(unsigned)__builtin_amdgcn_readfirstlane((unsigned)(e >> 32)) << 32) | (unsigned)__builtin_amdgcn_readfirstlane((unsigned)e)) + 1;
}
DI void publish(char* ws, unsigned flag_off, u64 tag) {
    WAIT_VM(0);
    BAR();
    if (threadIdx.x < 8) __hip_atomic_store((gu64*)(ws + flag_off + threadIdx.x * 8), tag, RLX_AGENT);
}
DI void publish_wave(char* ws, unsigned flag_off, u64 tag) {
    WAIT_VM(0);
    if ((threadIdx.x & 63) < 8) __hip_atomic_store((gu64*)(ws + flag_off + (threadIdx.x & 63) * 8), tag, RLX_AGENT);
}
DI void await_wave(char* ws, unsigned off, u64 tag) {
    _Pragma("unroll 1") for (unsigned spins = 0; spins < SPIN_CAP; ++spins) {
        if (__all(__hip_atomic_load((gu64*)(ws + off), RLX_AGENT) == tag)) break;
        __builtin_amdgcn_s_sleep(POLL_SLEEP);
    }
    asm volatile("" ::: "memory");
}
DI void poll2(char* ws, unsigned off0, unsigned off1, u64 tag) {
    _Pragma("unroll 1") for (unsigned spins = 0; spins < SPIN_CAP; ++spins) {
        const u64 a = __hip_atomic_load((gu64*)(ws + off0), RLX_AGENT), b = __hip_atomic_load((gu64*)(ws + off1), RLX_AGENT);
        if (__all(a == tag && b == tag)) break;
        __builtin_amdgcn_s_sleep(POLL_SLEEP);
    }
}
DI void await2(char* ws, unsigned off0, unsigned off1, u64 tag, int wv) {
    if (wv == 0) poll2(ws, off0, off1, tag);
    asm volatile("s_waitcnt lgkmcnt(0)\n\ts_barrier" ::: "memory");
}

template <int KIND>
DI u64 qkv_body(const bf16* x, const bf16* wqkv, int m0, int nt, int k0, char* ws, unsigned lds0, int wv, int bid) {
    const int l = threadIdx.x & 63, wn = wv & 3, wm = wv >> 2, i16 = l & 15, g = l >> 4;
    const rsrc_t rx = mk_rsrc(x, S_ * D_ * 2), rw = mk_rsrc(wqkv, D_ * 1920 * 2);
    unsigned xoff, woff;
    {
        const int rho = 8 * wv + (l >> 3), i = rho & 15;
        const int s = KIND == 2 ? (rho & ~31) + 8 * (i >> 2) + 4 * ((rho >> 4) & 1) + (i & 3) : rho;
        xoff = (unsigned)((m0 + s) * (D_ * 2) + (((l & 7) ^ (l >> 3)) << 4));
        const int k = 4 * wv + (l >> 4), f = (k & 3) | (((k >> 3) & 1) << 2);
        woff = (unsigned)(k * 3840 + nt * 256 + (((i16 >> 1) ^ f) << 5) + ((i16 & 1) << 4));
    }
    auto piece = [&](int u, int i) {
        const unsigned dst = lds0 + (u % 5) * 16384 + i * 8192 + wv * 1024;
        const int ks = k0 + (u >> 1) - (k0 + (u >> 1) >= 10 ? 10 : 0);
        if (u & 1) dma16(dst, woff, rw, (ks * 64 + i * 32) * 3840);
        else dma16(dst, xoff, rx, ks * 128 + i * 64 * (D_ * 2));
    };
    TDECL; TREC(0);
#pragma unroll
    for (int u = 0; u < 5; ++u) { piece(u, 0); piece(u, 1); if (u == 1) BAR(); }
    const unsigned xrd = lds0 + (wm * 64 + i16) * 128 + ((g ^ (i16 & 7)) << 4);
    const unsigned wrd = lds0 + (8 * g + (i16 >> 2)) * 256 + (((2 * wn) ^ ((i16 >> 2) | ((g & 1) << 2))) << 5) + 8 * (l & 3);
    f32x4 acc[4][2];
#pragma unroll
    for (int m = 0; m < 4; ++m) { acc[m][0] = f32x4{0.f, 0.f, 0.f, 0.f}; acc[m][1] = f32x4{0.f, 0.f, 0.f, 0.f}; }
    bf16x8 xf[2][4], wf[2][2];
    u64 ep;
    auto rd1 = [&](int kt, int kk, int i) {
        const unsigned xs = ((2 * kt) % 5) * 16384, wsl = ((2 * kt + 1) % 5) * 16384;
        if (i < 2) { const unsigned a = (wrd ^ (i << 5)) + wsl + kk * 8192; wf[kk][i] = lds_tr2(a, a + 1024); }
        else if (i < 6) xf[kk][i - 2] = lds_rd16((xrd ^ (kk << 6)) + xs + (i - 2) * 2048);
    };
    auto mm = [&](int kk, int m) {
#pragma unroll
        for (int j = 0; j < 2; ++j) acc[m][j] = (KIND == 1) ? MFMA16(wf[kk][j], xf[kk][m], acc[m][j]) : MFMA16(xf[kk][m], wf[kk][j], acc[m][j]);
    };
#pragma unroll
    for (int kt = 0; kt < 10; ++kt) {
        TREC(3 * kt + 1);
        if (kt == 0) WAIT_VM_LGKM0(6); else if (kt < 9) WAIT_VM_LGKM0(2); else WAIT_VM_LGKM0(0);
        TREC(3 * kt + 2);
        BAR();
        TREC(3 * kt + 3);
        if (kt == 4) ep = epoch_load(ws);
        SB();
#pragma unroll
        for (int m = 0; m < 4; ++m) {
            if (kt > 0) mm(1, m);
            SB();
            rd1(kt, 0, 2 * m); rd1(kt, 0, 2 * m + 1);
            if (kt > 0 && 2 * kt + 3 + (m >> 1) < 20) piece(2 * kt + 3 + (m >> 1), m & 1);
            SB();
        }
#pragma unroll
        for (int m = 0; m < 4; ++m) {
            mm(0, m);
            SB();
            rd1(kt, 1, 2 * m); rd1(kt, 1, 2 * m + 1);
            SB();
        }
    }
#pragma unroll
    for (int m = 0; m < 4; ++m) mm(1, m);
    TREC(31); TDUMP(ws, 32);
    STAMP(ws, 1);
    const rsrc_t rws = mk_rsrc(ws, WS_BYTES);
    if (KIND == 2) {
#pragma unroll
        for (int j = 0; j < 2; ++j) {
            const int col = (nt - 10) * 128 + wn * 32 + 16 * j + i16;
            const int h = col / 20, d = col - h * 20;
            const int vp = d < 8 ? d : (d < 18 ? d - 8 : d - 10), vh = (d >= 4 && d < 12) || d == 16 || d == 17;
            const unsigned base = OFF_KV + (unsigned)(h * KV_HEAD + 8192 + vp * 1024 + vh * 512);
#pragma unroll
            for (int u = 0; u < 2; ++u) {
                const int s = m0 + wm * 64 + 32 * u + 8 * g;
                st16(rws, base + (s >> 8) * KV_CHUNK + (s & 255) * 2, pack8(acc[2 * u][j], acc[2 * u + 1][j]));
            }
        }
    } else {
        const bool isq = nt < 5;
        const float sc = isq ? 0.22360679774997896f * 1.4426950408889634f : 1.f;
        const int pos = isq ? (l & 31) : swap23(l & 31);
#pragma unroll
        for (int j = 0; j < 2; ++j) {
            const int c = (isq ? nt : nt - 5) * 128 + wn * 32 + 16 * j + 8 * (l >> 5);
            const int pair = c / 40, oc = (c - pair * 40) >> 3;
            const int h = 2 * pair + (oc > 2), half = oc > 2 ? oc - 3 : oc;
#pragma unroll
            for (int u = 0; u < 2; ++u) {
                const f32x4 a = acc[2 * u][j] * sc, b = acc[2 * u + 1][j] * sc;
                const auto r0 = __builtin_amdgcn_permlane16_swap(pk(a[0], a[1]), pk(b[0], b[1]), false, false);
                const auto r1 = __builtin_amdgcn_permlane16_swap(pk(a[2], a[3]), pk(b[2], b[3]), false, false);
                const int tile = (m0 + wm * 64 + 32 * u) >> 5;
                unsigned off;
                if (oc == 2) off = (isq ? OFF_Q2 : OFF_K2) + pair * 32768 + (tile * 32 + pos) * 16;
                else if (isq) off = OFF_Q1 + h * 65536 + tile * 1024 + (half * 32 + pos) * 16;
                else off = OFF_KV + h * KV_HEAD + (tile >> 3) * KV_CHUNK + (tile & 7) * 1024 + (half * 32 + pos) * 16;
                st16(rws, off, u32x4{r0[0], r1[0], r0[1], r1[1]});
            }
        }
    }
    WAIT_VM(0);
    return ep;
}

template <int WN>
DI u64 ab_body(const bf16* x, const bf16* w, bool isb, int m0, int n0, int k0, char* ws, unsigned lds0, int wv, int bid) {
    constexpr int NJ = WN == 0 ? 3 : 2, STG = 26624;
    const int l = threadIdx.x & 63, wm = wv & 3, i16 = l & 15, g = l >> 4;
    const rsrc_t rx = mk_rsrc(x, S_ * D_ * 2), rw = mk_rsrc(w, D_ * D_ * 2);
    unsigned xoff, woff;
    {
        const int rho = 8 * wv + (l >> 3), i = rho & 15;
        const int s = (rho & ~31) + 8 * (i >> 2) + 4 * ((rho >> 4) & 1) + (i & 3);
        xoff = (unsigned)((m0 + s) * (D_ * 2) + (((l & 7) ^ (l >> 3)) << 4));
        woff = (unsigned)((n0 + rho) * (D_ * 2) + (((l & 7) ^ (l >> 3)) << 4));
    }
    const bool extra = WN == 1 && wv < 6;
    auto piece = [&](int kt, int i) {
        const unsigned dst = lds0 + (kt % 3) * STG + wv * 1024;
        const int ks = k0 + kt - (k0 + kt >= 10 ? 10 : 0);
        if (i < 2) dma16(dst + i * 8192, xoff, rx, ks * 128 + i * 64 * (D_ * 2));
        else if (i == 2) dma16(dst + 16384, woff, rw, ks * 128);
        else if (WN == 1) { if (extra) dma16(dst + 16384 + 4096, woff, rw, ks * 128 + 32 * (D_ * 2)); }
    };
#pragma unroll
    for (int st = 0; st < 2; ++st)
#pragma unroll
        for (int i = 0; i < 4; ++i) piece(st, i);
    const unsigned xrd = lds0 + (wm * 32 + i16) * 128 + ((g ^ (i16 & 7)) << 4);
    const unsigned wrd = lds0 + 16384 + (WN * 48 + i16) * 128 + ((g ^ (i16 & 7)) << 4);
    f32x4 acc[2][NJ];
#pragma unroll
    for (int j = 0; j < NJ; ++j) { acc[0][j] = f32x4{0.f, 0.f, 0.f, 0.f}; acc[1][j] = f32x4{0.f, 0.f, 0.f, 0.f}; }
    bf16x8 xf[2][2], wf[2][NJ];
    u64 ep;
    auto rd1 = [&](int kt, int kk, int i) {
        const unsigned so = (kt % 3) * STG;
        if (i < 2) xf[kk][i] = lds_rd16((xrd ^ (kk << 6)) + so + i * 2048);
        else if (i - 2 < NJ) wf[kk][i - 2] = lds_rd16((wrd ^ (kk << 6)) + so + (i - 2) * 2048);
    };
    auto mm = [&](int kk, int j) {
#pragma unroll
        for (int m = 0; m < 2; ++m) acc[m][j] = MFMA16(xf[kk][m], wf[kk][j], acc[m][j]);
    };
#pragma unroll
    for (int kt = 0; kt < 10; ++kt) {
        if (kt < 9) { if (extra) WAIT_VM_LGKM0(4); else WAIT_VM_LGKM0(3); } else WAIT_VM_LGKM0(0);
        BAR();
        if (kt == 4) ep = epoch_load(ws);
        SB();
#pragma unroll
        for (int j = 0; j < NJ; ++j) {
            if (kt > 0) mm(1, j);
            SB();
            rd1(kt, 0, 2 * j); rd1(kt, 0, 2 * j + 1);
            if (kt + 2 < 10 && j < 2) piece(kt + 2, j);
            SB();
        }
#pragma unroll
        for (int j = 0; j < NJ; ++j) {
            mm(0, j);
            SB();
            rd1(kt, 1, 2 * j); rd1(kt, 1, 2 * j + 1);
            if (kt + 2 < 10 && j < 2) piece(kt + 2, 2 + j);
            SB();
        }
    }
#pragma unroll
    for (int j = 0; j < NJ; ++j) mm(1, j);
    STAMP(ws, 1);
    const rsrc_t rws = mk_rsrc(ws, WS_BYTES);
    const float sg = isb ? -1.f : 1.f;
    const int kg = (isb ? 2048 : 0) + m0 + 32 * wm + 8 * g;
#pragma unroll
    for (int j = 0; j < NJ; ++j) {
        const int e = n0 + WN * 48 + 16 * j + i16;
        const int ntile = e / 160, er = e - ntile * 160;
        st16(rws, OFF_AB + (unsigned)(ntile * 64 * 20480 + (er >> 5) * 1024 + swap23(er & 31) * 16 + (kg >> 4) * 5120 + ((kg >> 3) & 1) * 512),
             pack8(acc[0][j] * sg, acc[1][j] * sg));
    }
    WAIT_VM(0);
    return ep;
}

constexpr int AT_SLOT = 23728;

DI float half_swap_max(float m) {
    auto r = __builtin_amdgcn_permlane32_swap(__float_as_uint(m), __float_as_uint(m), false, false);
    return fmaxf(__uint_as_float(r[0]), __uint_as_float(r[1]));
}
DI float max16(const f32x16& s) {
    float a = fmaxf(fmaxf(s[0], s[1]), s[2]), b = fmaxf(fmaxf(s[3], s[4]), s[5]);
    a = fmaxf(fmaxf(a, s[6]), s[7]); b = fmaxf(fmaxf(b, s[8]), s[9]);
    a = fmaxf(fmaxf(a, s[10]), s[11]); b = fmaxf(fmaxf(b, s[12]), s[13]);
    a = fmaxf(fmaxf(a, s[14]), s[15]);
    return fmaxf(a, b);
}

DI void attn_phase(int head, int qb, char* ws, unsigned lds0, int wv, u64 tag, unsigned f0, unsigned f1, int bid) {
    const int l = threadIdx.x & 63, hh = l >> 5, r32 = l & 31;
    const int qt = qb * 8 + wv, pair = head >> 1, odd = head & 1;
    const rsrc_t rws = mk_rsrc(ws, WS_BYTES);
    const unsigned lane16 = l * 16;
    const unsigned kvsrc = OFF_KV + head * KV_HEAD + wv * 1024;
    const unsigned src3 = wv < 2 ? kvsrc + 16384 : OFF_K2 + pair * 32768 + (wv - 2) * 1024, step3 = wv < 2 ? KV_CHUNK : 4096;
    const unsigned dst3 = wv < 2 ? 12288 + (8 + wv) * 1040 : 8192 + (wv - 2) * 1024;
    auto load_chunk = [&](int c, int slot) {
        const unsigned dst = lds0 + slot * AT_SLOT;
        const int cc = (c + qb) & 7;
        dma16c(dst + wv * 1024, lane16, rws, kvsrc + cc * KV_CHUNK);
        dma16c(dst + 12288 + wv * 1040, lane16, rws, kvsrc + cc * KV_CHUNK + 8192);
        if (wv < 6) dma16c(dst + dst3, lane16, rws, src3 + cc * step3);
    };
    load_chunk(0, 0);
    const bf16x8 q1 = __builtin_bit_cast(bf16x8, ld16(rws, OFF_Q1 + head * 65536 + qt * 1024 + lane16));
    const u32x4 qr = ld16(rws, OFF_Q2 + pair * 32768 + (qt * 32 + r32) * 16);
    load_chunk(1, 1);
    if (wv < 3) {
        const unsigned c = hh ? 0u : 0x3F803F80u;
        *(LDSP u32x4*)(lds0 + wv * AT_SLOT + 12288 + 10400 + lane16 + hh * 16) = u32x4{c, c, c, c};
    }
    bf16x8 q2[2];
#pragma unroll
    for (int P = 0; P < 2; ++P) {
        const bool lo = hh == P && !odd, hi = hh == P && odd;
        q2[P] = __builtin_bit_cast(bf16x8, u32x4{lo ? qr[0] : 0u, lo ? qr[1] : 0u, hi ? qr[2] : 0u, hi ? qr[3] : 0u});
    }
    const bool vdata = r32 < 18 || r32 == 20 || r32 == 21, vone = r32 == 18 || r32 == 22;
    const int vp = r32 < 4 ? r32 : (r32 < 12 ? r32 - 4 : (r32 < 18 ? r32 - 8 : r32 - 12)), vh = (r32 >= 4 && r32 < 12) || r32 == 16 || r32 == 17;
    const unsigned voff = 12288 + hh * 16 + (vdata ? vp * 1040 + vh * 512 : (vone ? 10400 : 10928));
    const f32x16 zero16 = {0.f, 0.f, 0.f, 0.f, 0.f, 0.f, 0.f, 0.f, 0.f, 0.f, 0.f, 0.f, 0.f, 0.f, 0.f, 0.f};
    WAIT_VM_LGKM0(2);
    BAR();
    STAMP(ws, 4);
    unsigned kb = lds0 + lane16, vb = lds0 + voff;
    bf16x8 k2f = lds_rd16(kb + 8192);
    f32x16 sc = MFMA32(lds_rd16(kb), q1, zero16);
    sc = MFMA32(k2f, q2[0], sc);
    const float m0 = half_swap_max(max16(sc));
    f32x16 negm;
#pragma unroll
    for (int i = 0; i < 16; ++i) { negm[i] = -m0; sc[i] -= m0; }
    f32x16 o = zero16;
    bf16x8 k1f = lds_rd16(kb + 1024);
    bf16x8 v0f = lds_rd16(vb), v1f = lds_rd16(vb);
    u32x4 p0, p1 = {0u, 0u, 0u, 0u};
    float e12 = 0.f, e13 = 0.f, e14 = 0.f, e15 = 0.f;
    int slot = 0;
    for (int c = 0; c < 8; ++c) {
        const int nslot = slot == 2 ? 0 : slot + 1;
        const unsigned kbn = lds0 + nslot * AT_SLOT + lane16, vbn = lds0 + nslot * AT_SLOT + voff;
#pragma unroll
        for (int t = 0; t < 8; ++t) {
            if (t == 6) {
                if (c == 0 && wv == 0) poll2(ws, f0, f1, tag);
                WAIT_VM(0);
                BAR();
                if (c + 2 < 8) load_chunk(c + 2, nslot == 2 ? 0 : nslot + 1);
            }
            float a0, a1, a2, a3, b0, b1, b2, b3;
            SB();
            f32x16 sn = MFMA32(k1f, q1, negm);
            k1f = (t < 6) ? lds_rd16(kb + (t + 2) * 1024) : lds_rd16(kbn + (t - 6) * 1024);
            p1[2] = pk(e12, e13); p1[3] = pk(e14, e15); PIN(p1);
            a0 = EX2(sc[0]); a1 = EX2(sc[1]); a2 = EX2(sc[2]); a3 = EX2(sc[3]); PIN(a0); PIN(a1); PIN(a2); PIN(a3);
            SB();
            sn = MFMA32(k2f, q2[(t + 1) & 1], sn);
            if ((t & 1) == 0) k2f = (t < 6) ? lds_rd16(kb + 8192 + (t + 2) * 512) : lds_rd16(kbn + 8192);
            p0[0] = pk(a0, a1); p0[1] = pk(a2, a3);
            b0 = EX2(sc[4]); b1 = EX2(sc[5]); b2 = EX2(sc[6]); b3 = EX2(sc[7]); PIN(b0); PIN(b1); PIN(b2); PIN(b3);
            SB();
            o = MFMA32(v1f, __builtin_bit_cast(bf16x8, p1), o);
            v1f = lds_rd16(vb + t * 64 + 32);
            p0[2] = pk(b0, b1); p0[3] = pk(b2, b3); PIN(p0);
            a0 = EX2(sc[8]); a1 = EX2(sc[9]); a2 = EX2(sc[10]); a3 = EX2(sc[11]); PIN(a0); PIN(a1); PIN(a2); PIN(a3);
            SB();
            o = MFMA32(v0f, __builtin_bit_cast(bf16x8, p0), o);
            v0f = (t < 7) ? lds_rd16(vb + (t + 1) * 64) : lds_rd16(vbn);
            p1[0] = pk(a0, a1); p1[1] = pk(a2, a3);
            e12 = EX2(sc[12]); e13 = EX2(sc[13]); e14 = EX2(sc[14]); e15 = EX2(sc[15]); PIN(e12); PIN(e13); PIN(e14); PIN(e15);
            SB();
            sc = sn;
        }
        slot = nslot; kb = kbn; vb = vbn;
    }
    p1[2] = pk(e12, e13); p1[3] = pk(e14, e15);
    o = MFMA32(v1f, __builtin_bit_cast(bf16x8, p1), o);
    STAMP(ws, 5);
    float asum = 0.f;
#pragma unroll
    for (int i = 0; i < 10; ++i) asum += fabsf(o[i]);
    const bool bad = !(o[10] <= 1.0e18f) || !(asum <= 1.0e37f);
    if (__builtin_expect(__any(bad), 0)) {
        const unsigned vg = 8192 + hh * 16 + (vdata ? vp * 1024 + vh * 512 : 0), vc = vone ? 0x3F803F80u : 0u;
        const u32x4 vconst = {vc, vc, vc, vc};
        float m = -INFINITY;
        o = zero16;
        for (int t = 0; t < 64; ++t) {
            const unsigned cb = OFF_KV + head * KV_HEAD + (t >> 3) * KV_CHUNK;
            const int T = t & 7;
            f32x16 s = MFMA32(__builtin_bit_cast(bf16x8, ld16(rws, cb + T * 1024 + lane16)), q1, zero16);
            s = MFMA32(__builtin_bit_cast(bf16x8, ld16(rws, OFF_K2 + pair * 32768 + (t * 32 + r32) * 16)), q2[0], s);
            const float mn = fmaxf(m, half_swap_max(max16(s)));
            const float al = EX2(m - mn);
            m = mn;
#pragma unroll
            for (int i = 0; i < 16; ++i) { o[i] *= al; s[i] = EX2(s[i] - mn); }
            const u32x4 pa = {pk(s[0], s[1]), pk(s[2], s[3]), pk(s[4], s[5]), pk(s[6], s[7])};
            const u32x4 pb = {pk(s[8], s[9]), pk(s[10], s[11]), pk(s[12], s[13]), pk(s[14], s[15])};
            const u32x4 va = ld16(rws, cb + vg + T * 64), vb2 = ld16(rws, cb + vg + T * 64 + 32);
            o = MFMA32(__builtin_bit_cast(bf16x8, vdata ? va : vconst), __builtin_bit_cast(bf16x8, pa), o);
            o = MFMA32(__builtin_bit_cast(bf16x8, vdata ? vb2 : vconst), __builtin_bit_cast(bf16x8, pb), o);
        }
    }
    const float rl = 1.f / o[10];
    const int q = qt * 32 + r32;
    st16(rws, OFF_OM + ((q >> 4) * 16 + pair) * 1024 + (odd * 2 + hh) * 256 + (q & 15) * 16,
         u32x4{pk(o[0] * rl, o[1] * rl), pk(o[2] * rl, o[3] * rl), pk(o[4] * rl, o[5] * rl), pk(o[6] * rl, o[7] * rl)});
    const unsigned xl = pk(o[8] * rl, o[9] * rl);
    const auto rx = __builtin_amdgcn_permlane32_swap(xl, xl, false, false);
    if (!hh) st8(rws, OFF_OL + (head * S_ + q) * 8, u32x2{rx[0], rx[1]});
}

template <int NE>
DI void dft_body(unsigned lds0, int wv, int e0, rsrc_t rc, rsrc_t rws, unsigned csrc, unsigned absrc, int kstart, unsigned ydst) {
    const int l = threadIdx.x & 63, hh = l >> 5, r32 = l & 31, wm = wv & 3;
    const unsigned lane16 = l * 16;
    const unsigned coff = (unsigned)((8 * wv + (l >> 3)) * 4096 + (((l & 7) ^ ((4 * wv + (l >> 4)) & 7)) << 4));
    auto piece = [&](int i, int slot, int ks) {
        const unsigned base = lds0 + slot * 36864 + wv * 1024;
        const unsigned as = absrc + ks * 20480 + wv * 1024;
        if (i == 0) dma16(base, coff, rc, csrc + ks * 128);
        else if (i == 1) dma16(base + 8192, coff, rc, csrc + ks * 128 + 64 * 4096);
        else if (i == 2) dma16c(base + 16384, lane16, rws, as);
        else if (i == 3) dma16c(base + 16384 + 8192, lane16, rws, as + 8192);
        else if (NE == 2) dma16c(base + 16384 + 12288, lane16, rws, as + 12288);
    };
    int ksn = kstart;
#pragma unroll
    for (int i = 0; i < 5; ++i) piece(i, 0, ksn);
    const int row = wm * 32 + r32;
    const unsigned crd = lds0 + row * 128 + ((hh ^ ((row >> 1) & 7)) << 4);
    const unsigned ard = lds0 + 16384 + e0 * 1024 + lane16;
    f32x16 acc[NE];
#pragma unroll
    for (int e = 0; e < NE; ++e)
#pragma unroll
        for (int i = 0; i < 16; ++i) acc[e][i] = 0.f;
    bf16x8 cf[2], af[2][NE];
    const bf16x8 z8 = {0, 0, 0, 0, 0, 0, 0, 0};
    cf[1] = z8;
#pragma unroll
    for (int e = 0; e < NE; ++e) af[1][e] = z8;
    auto quarter = [&](int kk, unsigned so, bool pre, int slot) {
#pragma unroll
        for (int e = 0; e < NE; ++e) {
            acc[e] = MFMA32(af[(kk + 1) & 1][e], cf[(kk + 1) & 1], acc[e]);
            SB();
            if (e == 0) cf[kk & 1] = lds_rd16((crd ^ (kk << 5)) + so);
            af[kk & 1][e] = lds_rd16(ard + so + (kk * 5 + e) * 1024);
            if (pre && e < 2 && kk * 2 + e < 5) piece(kk * 2 + e, slot, ksn);
            SB();
        }
    };
#pragma unroll 2
    for (int ks = 0; ks < 16; ++ks) {
        WAIT_VM_LGKM0(0);
        BAR();
        if (ks == DFT_HI) __builtin_amdgcn_s_setprio(DFT_PRIO);
        ksn = (ksn + 1) & 15;
        const unsigned so = (ks & 1) * 36864;
        const bool pre = ks + 1 < 16;
        SB();
#pragma unroll
        for (int kk = 0; kk < 4; ++kk) quarter(kk, so, pre, (ks + 1) & 1);
    }
    __builtin_amdgcn_s_setprio(G_PRIO);
#pragma unroll
    for (int e = 0; e < NE; ++e) acc[e] = MFMA32(af[1][e], cf[1], acc[e]);
#pragma unroll
    for (int e = 0; e < NE; ++e)
#pragma unroll
        for (int s = 0; s < 2; ++s) {
            const u32x4 v = {pk(acc[e][8 * s], acc[e][8 * s + 1]), pk(acc[e][8 * s + 2], acc[e][8 * s + 3]),
                             pk(acc[e][8 * s + 4], acc[e][8 * s + 5]), pk(acc[e][8 * s + 6], acc[e][8 * s + 7])};
            st16(rws, ydst + ((e0 + e) * 2 + s) * 1024 + lane16, v);
        }
}

DI void final_phase(int mt, int nt, const bf16* wo, char* ws, bf16* out, unsigned lds0, int wv, u64 tag, int bid) {
    const int l = threadIdx.x & 63, kq = wv & 3, th = wv >> 2, i16 = l & 15, g = l >> 4;
    const rsrc_t rwo = mk_rsrc(wo, D_ * D_ * 2), rws = mk_rsrc(ws, WS_BYTES);
    const unsigned wsrc = (unsigned)(kq * 160 * (D_ * 2) + nt * 160);
    const unsigned wrd = lds0 + kq * 20480 + ((g >> 1) * 16 + (g & 1) * 4 + (i16 >> 2)) * 160 + 8 * (l & 3);
#pragma unroll
    for (int i = 0; i < 3; ++i) {
        const int c = (th * 3 + i) * 64 + l, rho = c / 10, ka = swap23(rho);
        if (th == 0 || i < 2) dma16(lds0 + kq * 20480 + (th * 3 + i) * 1024, (unsigned)((20 * (ka >> 2) + 16 + (ka & 3)) * (D_ * 2) + (c - rho * 10) * 16), rwo, wsrc);
    }
    unsigned woff[5];
#pragma unroll
    for (int i = 0; i < 5; ++i) {
        const int c = i * 64 + l, rho = c / 10, ka = swap23(rho);
        woff[i] = (unsigned)((20 * (ka >> 4) + (ka & 15)) * (D_ * 2) + (c - rho * 10) * 16);
    }
    WAIT_VM(0);
    BAR();
    bf16x8 w4[5];
#pragma unroll
    for (int e = 0; e < 5; ++e) w4[e] = lds_tr2(wrd + e * 32, wrd + e * 32 + 1280);
    asm volatile("s_waitcnt lgkmcnt(0)\n\ts_barrier" ::: "memory");
#pragma unroll
    for (int b = 0; b < 2; ++b)
#pragma unroll
        for (int i = 0; i < 5; ++i) dma16(lds0 + kq * 20480 + (th * 2 + b) * 5120 + i * 1024, woff[i], rwo, wsrc + (th * 2 + b) * 40 * (D_ * 2));
    await2(ws, OFF_FG2 + (((mt >> 1) * 4 + (nt >> 1)) * 4 + (l & 3)) * 64, OFF_FG3 + (mt * 8 + (l & 7)) * 64, tag, wv);
    STAMP(ws, 6);
    u32x4 yq[2]; float gv[2];
#pragma unroll
    for (int it = 0; it < 2; ++it) {
        if (it == 1 && wv >= 2) break;
        const int item = wv + 8 * it, t32 = item / 5, e16 = item - t32 * 5;
        u32x4 yv[4];
#pragma unroll
        for (int p = 0; p < 4; ++p) yv[p] = ld16(rws, OFF_Y + (unsigned)(((p * 64 + mt * 2 + t32) * 40 + nt * 5 + e16) * 1024 + l * 16));
        gv[it] = __uint_as_float(__builtin_amdgcn_raw_buffer_load_b32(rws, OFF_G + (mt * 64 + t32 * 32 + (l & 31)) * 64, 0, 16));
#pragma unroll
        for (int j = 0; j < 4; ++j)
            yq[it][j] = pk((bf_lo(yv[0][j]) + bf_lo(yv[1][j])) + (bf_lo(yv[2][j]) + bf_lo(yv[3][j])),
                           (bf_hi(yv[0][j]) + bf_hi(yv[1][j])) + (bf_hi(yv[2][j]) + bf_hi(yv[3][j])));
        PIN(yq[it]);
    }
    WAIT_VM(0);
    STAMP(ws, 7);
    BAR();
    await_wave(ws, OFF_FA2 + ((mt * 2 + th) * 32 + kq * 8 + (l & 7)) * 64, tag);
    STAMP(ws, 8);
    bf16x8 of[5][2];
#pragma unroll
    for (int k = 0; k < 4; ++k)
#pragma unroll
        for (int t = 0; t < 2; ++t)
            of[k][t] = __builtin_bit_cast(bf16x8, ld16(rws, OFF_OM + (unsigned)(((mt * 4 + th * 2 + t) * 16 + kq * 4 + k) * 1024 + l * 16)));
#pragma unroll
    for (int t = 0; t < 2; ++t) {
        const unsigned off = OFF_OL + (unsigned)(((kq * 8 + 2 * g) * S_ + mt * 64 + th * 32 + t * 16 + i16) * 8);
        const u32x2 lo = __builtin_amdgcn_raw_buffer_load_b64(rws, off, 0, 16), hi = __builtin_amdgcn_raw_buffer_load_b64(rws, off + S_ * 8, 0, 16);
        of[4][t] = __builtin_bit_cast(bf16x8, u32x4{lo[0], lo[1], hi[0], hi[1]});
    }
    SB();
    f32x4 acc[5][2];
#pragma unroll
    for (int e = 0; e < 5; ++e) { acc[e][0] = f32x4{0.f, 0.f, 0.f, 0.f}; acc[e][1] = f32x4{0.f, 0.f, 0.f, 0.f}; }
#pragma unroll
    for (int k = 0; k < 5; ++k)
#pragma unroll
        for (int e = 0; e < 5; ++e) {
            const bf16x8 w = k < 4 ? lds_tr2(wrd + k * 5120 + e * 32, wrd + k * 5120 + e * 32 + 1280) : w4[e];
#pragma unroll
            for (int t = 0; t < 2; ++t) acc[e][t] = MFMA16(w, of[k][t], acc[e][t]);
        }
    asm volatile("s_waitcnt lgkmcnt(0)\n\ts_barrier" ::: "memory");
#pragma unroll
    for (int e = 0; e < 5; ++e)
#pragma unroll
        for (int t = 0; t < 2; ++t)
            *(LDSP f32x4*)(lds0 + (kq * 64 + th * 32 + t * 16 + i16) * 320 + e * 64 + ((g ^ (i16 >> 2)) << 4)) = acc[e][t];
    asm volatile("s_waitcnt lgkmcnt(0)\n\ts_barrier" ::: "memory");
#pragma unroll
    for (int it = 0; it < 2; ++it) {
        if (it == 1 && wv >= 2) break;
        const int item = wv + 8 * it, t32 = item / 5, e16 = item - t32 * 5;
        const int tl = t32 * 32 + (l & 31), sw = (l >> 2) & 3, c0 = (l >> 5) * 2;
        f32x4 s0 = {0.f, 0.f, 0.f, 0.f}, s1 = {0.f, 0.f, 0.f, 0.f};
#pragma unroll
        for (int p = 0; p < 4; ++p) {
            s0 += *(const LDSP f32x4*)(lds0 + (p * 64 + tl) * 320 + e16 * 64 + ((c0 ^ sw) << 4));
            s1 += *(const LDSP f32x4*)(lds0 + (p * 64 + tl) * 320 + e16 * 64 + (((c0 + 1) ^ sw) << 4));
        }
        const float gg = gv[it], g1 = 1.f - gg;
        const u32x4 y = yq[it];
        const u32x4 v = {pk(gg * bf_lo(y[0]) + g1 * s0[0], gg * bf_hi(y[0]) + g1 * s0[1]), pk(gg * bf_lo(y[1]) + g1 * s0[2], gg * bf_hi(y[1]) + g1 * s0[3]),
                         pk(gg * bf_lo(y[2]) + g1 * s1[0], gg * bf_hi(y[2]) + g1 * s1[1]), pk(gg * bf_lo(y[3]) + g1 * s1[2], gg * bf_hi(y[3]) + g1 * s1[3])};
        st16(mk_rsrc(out, S_ * D_ * 2), (unsigned)((mt * 64 + tl) * (D_ * 2) + (nt * 80 + e16 * 16 + (l >> 5) * 8) * 2), v);
    }
}

template <int PH>
__global__ __launch_bounds__(512, 4) void fused_kernel(const bf16* x, const bf16* cr, const bf16* ci, const bf16* wr, const bf16* wi,
                                                       const bf16* wqkv, const bf16* wo, const bf16* gw, char* ws, bf16* out) {
    u64 t_entry; asm volatile("s_memrealtime %0\n\ts_waitcnt lgkmcnt(0)" : "=s"(t_entry));
    extern __shared__ __attribute__((aligned(16))) char smem[];
    const unsigned lds0 = (unsigned)(uintptr_t)(LDSP char*)smem;
    const int bid = blockIdx.x ^ (SWAP_ROLES << 8), b = bid & 255, xcd = b & 7, slot = b >> 3, l = threadIdx.x & 63;
    const int wv = __builtin_amdgcn_readfirstlane(threadIdx.x >> 6);
    u64 tag = 0;
    const int mt1 = (xcd & 1) * 8 + (slot & 7), nt1 = (xcd >> 1) * 4 + (slot >> 3);
    const int k0 = ROT ? (2 * (slot & 7) + (((slot >> 3) * 5 + 1) >> 1)) % 10 : 0;
    const char* pc;
    asm volatile("s_getpc_b64 %0" : "=s"(pc));
    if (PH & 1) STAMP(ws, 0);
    if (__builtin_expect(bid < 256, 1)) {
        if (PH & 1) {
            __builtin_amdgcn_s_setprio(A_PRIO);
            for (int i = 0; i < A_DELAY; ++i) __builtin_amdgcn_s_sleep(16);
            u64 ep;
            if (nt1 < 10) ep = qkv_body<1>(x, wqkv, mt1 * 128, nt1, k0, ws, lds0, wv, bid);
            else if (nt1 < 15) ep = qkv_body<2>(x, wqkv, mt1 * 128, nt1, k0, ws, lds0, wv, bid);
            else ep = epoch_load(ws);
            tag = epoch_tag(ep);
            publish(ws, OFF_FA1 + (nt1 * 16 + mt1) * 64, tag);
            STAMP(ws, 2);
        }
        if (PH & 2) {
            const int head = xcd * 4 + (slot >> 3), qb = slot & 7;
            const int n0 = ((head >> 1) * 40) >> 7, n1 = ((head >> 1) * 40 + 39) >> 7;
            const int sec = (l >> 2) & 3, sb = sec == 3 ? 0 : sec * 5;
            const int mrow = (qb * 2 + (sec == 1 || sec == 2 ? (l & 3) : (l & 1))) & 15;
            await2(ws, OFF_FA1 + ((sb + n0) * 16 + mrow) * 64, OFF_FA1 + ((sb + n1) * 16 + mrow) * 64, tag, wv);
            STAMP(ws, 3);
            __builtin_amdgcn_s_setprio(ATT_PRIO);
            const int sb2 = 5 + ((l >> 4) & 1) * 5;
            attn_phase(head, qb, ws, lds0, wv, tag, OFF_FA1 + ((sb2 + n0) * 16 + (l & 15)) * 64, OFF_FA1 + ((sb2 + n1) * 16 + (l & 15)) * 64, bid);
            publish_wave(ws, OFF_FA2 + ((qb * 8 + wv) * 32 + head) * 64, tag);
            if (bid == SPB) while (__builtin_amdgcn_s_memrealtime() - t_entry < 10000) __builtin_amdgcn_s_sleep(4);
            STAMP(ws, 9);
        }
    } else {
        const rsrc_t rws = mk_rsrc(ws, WS_BYTES);
        __builtin_amdgcn_s_setprio(G_PRIO);
        if (PH & 1) {
            {
                const unsigned td = lds0 + 79872 + wv * 256;
                if (wv == 0) touch(td, l < 10 ? (unsigned)((l * 32 + slot) * 128) : 0x7ffffff0u, mk_rsrc(pc, CODE_PF));
                const int id = slot * 8 + wv;
                const int dl = id * 13 + l;
                touch(td, l < 13 && dl < 3200 ? (unsigned)((((xcd >> 1) & 1) * 3200 + dl) * 128) : 0x7ffffff0u, mk_rsrc(xcd >= 4 ? wi : wr, D_ * D_ * 2));
            }
            for (int i = 0; i < G_DELAY0; ++i) __builtin_amdgcn_s_sleep(16);
            if (wv == 0) touch(lds0 + 79872, l < 25 ? (unsigned)((b * 25 + l) * 128) : 0x7ffffff0u, mk_rsrc(wo, D_ * D_ * 2));
            for (int i = G_DELAY0; i < G_DELAY; ++i) __builtin_amdgcn_s_sleep(16);
            const bool isb = nt1 >= 8;
            u64 ep;
            if (wv < 4) ep = ab_body<0>(x, isb ? wi : wr, isb, mt1 * 128, (nt1 & 7) * 80, k0, ws, lds0, wv, bid);
            else ep = ab_body<1>(x, isb ? wi : wr, isb, mt1 * 128, (nt1 & 7) * 80, k0, ws, lds0, wv, bid);
            tag = epoch_tag(ep);
            publish(ws, OFF_FG1 + (nt1 * 16 + mt1) * 64, tag);
            STAMP(ws, 2);
            const int row = b * 8 + wv;
            const unsigned* xr = (const unsigned*)(x + row * D_);
            const unsigned* gp = (const unsigned*)gw;
            unsigned xa[5], ga[5];
#pragma unroll
            for (int i = 0; i < 5; ++i) { xa[i] = xr[l + 64 * i]; ga[i] = gp[l + 64 * i]; }
            float s = 0.f;
#pragma unroll
            for (int i = 0; i < 5; ++i) s += bf_lo(xa[i]) * bf_lo(ga[i]) + bf_hi(xa[i]) * bf_hi(ga[i]);
#pragma unroll
            for (int off = 32; off > 0; off >>= 1) s += __shfl_xor(s, off);
            if (l < 16) st4(rws, OFF_G + row * 64 + l * 4, __float_as_uint(1.f / (1.f + __expf(-s))));
        }
        if (PH & 2) {
            const int kq = xcd & 3, mt = (xcd >> 2) * 8 + (slot >> 2), ntile = slot & 3;
            const unsigned fo = OFF_FG1 + (((kq >> 1) * 8 + ntile * 2) * 16 + (kq & 1) * 8 + (l & 7)) * 64;
            await2(ws, fo, fo + 1024, tag, wv);
            STAMP(ws, 3);
            const rsrc_t rc = mk_rsrc(kq < 2 ? cr : ci, S_ * S_ * 2);
            const unsigned csrc = (unsigned)(mt * 128 * 4096 + (kq & 1) * 2048);
            const unsigned absrc = OFF_AB + (unsigned)((ntile * 64 + kq * 16) * 20480);
            const unsigned ydst = OFF_Y + (unsigned)(((kq * 64 + mt * 4 + (wv & 3)) * 40 + ntile * 10) * 1024);
            const int kstart = (2 * (slot >> 2) + 4 * ntile + (ntile & 1)) & 15;
            if (wv < 4) dft_body<3>(lds0, wv, 0, rc, rws, csrc, absrc, kstart, ydst);
            else dft_body<2>(lds0, wv, 3, rc, rws, csrc, absrc, kstart, ydst);
            publish(ws, OFF_FG2 + ((mt * 4 + ntile) * 4 + kq) * 64, tag);
            if (threadIdx.x < 8) __hip_atomic_store((gu64*)(ws + OFF_FG3 + b * 64 + threadIdx.x * 8), tag, RLX_AGENT);
            STAMP(ws, 5);
        }
        if (PH & 4) {
            final_phase(xcd * 4 + (slot >> 3), slot & 7, wo, ws, out, lds0, wv, tag, bid);
            STAMP(ws, 9);
            if (b == 0 && wv == 0) {
                for (unsigned spins = 0; spins < (1u << 22); ++spins) {
                    bool ok = true;
#pragma unroll
                    for (int i = 0; i < 4; ++i)
                        ok = ok && __hip_atomic_load((gu64*)(ws + OFF_FA1 + (i * 64 + l) * 64), RLX_AGENT) == tag &&
                             __hip_atomic_load((gu64*)(ws + OFF_FG1 + (i * 64 + l) * 64), RLX_AGENT) == tag;
                    if (__all(ok)) break;
                    __builtin_amdgcn_s_sleep(2);
                }
                if (l < 8) __hip_atomic_store((gu64*)(ws + OFF_EPOCH + l * 8), tag, RLX_AGENT);
            }
        }
    }
}

extern "C" void kernel_launch(void* const* d_in, const int* in_sizes, int n_in, void* d_out, int out_size, void* d_ws,
                              size_t ws_size, hipStream_t stream) {
    const bf16* x = (const bf16*)d_in[0];
    const bf16* cr = (const bf16*)d_in[1];
    const bf16* ci = (const bf16*)d_in[2];
    const bf16* wr = (const bf16*)d_in[3];
    const bf16* wi = (const bf16*)d_in[4];
    const bf16* wqkv = (const bf16*)d_in[6];
    const bf16* wo = (const bf16*)d_in[7];
    const bf16* gw = (const bf16*)d_in[8];
    char* ws = (char*)d_ws;
    bf16* out = (bf16*)d_out;
    (void)hipFuncSetAttribute((const void*)fused_kernel<7>, hipFuncAttributeMaxDynamicSharedMemorySize, LDS_BYTES);
    fused_kernel<7><<<512, 512, LDS_BYTES, stream>>>(x, cr, ci, wr, wi, wqkv, wo, gw, ws, out);
}
```

```cpp
#include <hip/hip_runtime.h>
#include <hip/hip_bf16.h>
#include <stdint.h>

typedef short bf16x8 __attribute__((ext_vector_type(8)));
typedef short s16x4 __attribute__((ext_vector_type(4)));
typedef float f32x16 __attribute__((ext_vector_type(16)));
typedef float f32x4 __attribute__((ext_vector_type(4)));
typedef float f32x2 __attribute__((ext_vector_type(2)));
typedef unsigned u32x4 __attribute__((ext_vector_type(4)));
typedef unsigned u32x2 __attribute__((ext_vector_type(2)));
typedef __bf16 bf16x2_t __attribute__((ext_vector_type(2)));
typedef unsigned long long u64;
typedef __attribute__((address_space(1))) u64 gu64;
typedef __hip_bfloat16 bf16;
#define LDSP __attribute__((address_space(3)))
#define DI __device__ __forceinline__
typedef __amdgpu_buffer_rsrc_t rsrc_t;

constexpr int S_ = 2048, D_ = 640, H_ = 32;
constexpr unsigned OFF_EPOCH = 0;
constexpr unsigned OFF_FQ = 16384, OFF_FAB = 32768, OFF_FO = 49152, OFF_FG = 65536, OFF_FY = 131072;
constexpr unsigned OFF_G = 262144;
constexpr unsigned OFF_Q1 = 524288;
constexpr unsigned OFF_Q2 = OFF_Q1 + H_ * 65536;
constexpr unsigned OFF_K2 = OFF_Q2 + 16 * 32768;
constexpr unsigned KV_CHUNK = 18432, KV_HEAD = 8 * KV_CHUNK;
constexpr unsigned OFF_KV = OFF_K2 + 16 * 32768;
constexpr unsigned OFF_AB = OFF_KV + H_ * KV_HEAD;
constexpr unsigned OFF_Y = OFF_AB + 4 * 64 * 20480;
constexpr unsigned OFF_OM = OFF_Y + 4 * S_ * D_ * 2;
constexpr unsigned OFF_OL = OFF_OM + S_ * 512 * 2;
constexpr unsigned WS_BYTES = OFF_OL + H_ * S_ * 8;
constexpr int LDS_BYTES = 163840;
constexpr int RSLOT = 36864;
constexpr int AT_SLOT = 23744;
constexpr int Q_LDS = 4 * RSLOT, TOUCH_LDS = Q_LDS + 12288;
constexpr unsigned SPIN_CAP = 1u << 16;
constexpr unsigned OOB = 0x7ffffff0u;

#define STAMP(ws, i) do { } while (0)
#define MFMA32(a, b, c) __builtin_amdgcn_mfma_f32_32x32x16_bf16((a), (b), (c), 0, 0, 0)
#define MFMA16(a, b, c) __builtin_amdgcn_mfma_f32_16x16x32_bf16((a), (b), (c), 0, 0, 0)
#define WAIT_VM(n) asm volatile("s_waitcnt vmcnt(" #n ")" ::: "memory")
#define TL_LOG() do { } while (0)
#define VM_BAR(n) asm volatile("s_waitcnt vmcnt(" #n ")\n\ts_barrier" ::: "memory")
#define LGKM_BAR() asm volatile("s_waitcnt lgkmcnt(0)\n\ts_barrier" ::: "memory")
#define ALL_BAR() asm volatile("s_waitcnt vmcnt(0) lgkmcnt(0)\n\ts_barrier" ::: "memory")
#define EX2(v) __builtin_amdgcn_exp2f(v)
#define PIN(v) asm volatile("" : "+v"(v))
#define SB() __builtin_amdgcn_sched_barrier(0)
#define RLX_AGENT __ATOMIC_RELAXED, __HIP_MEMORY_SCOPE_AGENT
#define POLL_SLEEP 2
#define L_PRIO 1
#define DFT_RPG 2
#define PRIO_TRICK 1
#define PRIO_U 1
#define AB_RPG 2
#define QKV_ROT 5
#define QKV_DR 3
#define Y_DR 4
#define CODE_PF 36096

DI unsigned pk(float a, float b) { f32x2 v = {a, b}; return __builtin_bit_cast(unsigned, __builtin_convertvector(v, bf16x2_t)); }
DI float bf_lo(unsigned u) { return __uint_as_float(u << 16); }
DI float bf_hi(unsigned u) { return __uint_as_float(u & 0xffff0000u); }
DI rsrc_t mk_rsrc(const void* p, unsigned bytes) { return __builtin_amdgcn_make_buffer_rsrc((void*)p, (short)0, (int)bytes, 0x00020000); }
DI void dma16(unsigned lds_addr, unsigned voff, rsrc_t rs, unsigned soff) {
    asm volatile("s_mov_b32 m0, %0\n\ts_nop 0\n\tbuffer_load_dwordx4 %1, %2, %3 offen lds" ::"s"(lds_addr), "v"(voff), "s"(rs), "s"(soff) : "memory");
}
DI void dma16c(unsigned lds_addr, unsigned voff, rsrc_t rs, unsigned soff) {
    asm volatile("s_mov_b32 m0, %0\n\ts_nop 0\n\tbuffer_load_dwordx4 %1, %2, %3 offen sc1 lds" ::"s"(lds_addr), "v"(voff), "s"(rs), "s"(soff) : "memory");
}
DI void touch(unsigned lds_addr, unsigned voff, rsrc_t rs) {
    asm volatile("s_mov_b32 m0, %0\n\ts_nop 0\n\tbuffer_load_dword %1, %2, 0 offen sc1 lds" ::"s"(lds_addr), "v"(voff), "s"(rs) : "memory");
}
DI bf16x8 lds_rd16(unsigned a) { return *(const LDSP bf16x8*)(a); }
DI s16x4 lds_tr(unsigned a) { return __builtin_amdgcn_ds_read_tr16_b64_v4i16((LDSP s16x4*)(a)); }
DI bf16x8 lds_tr2(unsigned a0, unsigned a1) { return __builtin_shufflevector(lds_tr(a0), lds_tr(a1), 0, 1, 2, 3, 4, 5, 6, 7); }
DI void st16(rsrc_t rs, unsigned off, u32x4 v) { __builtin_amdgcn_raw_buffer_store_b128(v, rs, off, 0, 16); }
DI void st8(rsrc_t rs, unsigned off, u32x2 v) { __builtin_amdgcn_raw_buffer_store_b64(v, rs, off, 0, 16); }
DI void st4(rsrc_t rs, unsigned off, unsigned v) { __builtin_amdgcn_raw_buffer_store_b32(v, rs, off, 0, 16); }
DI u32x4 ld16(rsrc_t rs, unsigned off) { return __builtin_amdgcn_raw_buffer_load_b128(rs, off, 0, 16); }
DI int swap23(int i) { return (i & ~12) | ((i & 4) << 1) | ((i & 8) >> 1); }
DI u32x4 pack8(const f32x4& a, const f32x4& b) { return u32x4{pk(a[0], a[1]), pk(a[2], a[3]), pk(b[0], b[1]), pk(b[2], b[3])}; }
DI u32x4 pack8x(const f32x4& a, const f32x4& b) {
    const auto r0 = __builtin_amdgcn_permlane16_swap(pk(a[0], a[1]), pk(b[0], b[1]), false, false);
    const auto r1 = __builtin_amdgcn_permlane16_swap(pk(a[2], a[3]), pk(b[2], b[3]), false, false);
    return u32x4{r0[0], r1[0], r0[1], r1[1]};
}

DI u64 epoch_tag(char* ws) {
    u64 e;
    asm volatile("s_load_dwordx2 %0, %1, 0x0\n\ts_waitcnt lgkmcnt(0)" : "=s"(e) : "s"(ws + OFF_EPOCH) : "memory");
    return e + 1;
}
DI int lane_here() { int l = threadIdx.x & 63; PIN(l); return l; }
DI void set_flag(char* ws, unsigned off, u64 tag) {
    const int l = lane_here();
    if (l < 8) __hip_atomic_store((gu64*)(ws + off + l * 8), tag, RLX_AGENT);
}
DI void poll2(char* ws, unsigned off0, unsigned off1, u64 tag) {
    _Pragma("unroll 1") for (unsigned spins = 0; spins < SPIN_CAP; ++spins) {
        const u64 a = __hip_atomic_load((gu64*)(ws + off0), RLX_AGENT), b = __hip_atomic_load((gu64*)(ws + off1), RLX_AGENT);
        if (__all(a == tag && b == tag)) break;
        __builtin_amdgcn_s_sleep(POLL_SLEEP);
    }
    asm volatile("" ::: "memory");
}

DI u64 look2(char* ws, unsigned off0, unsigned off1, u64 tag) {
    const u64 a = __hip_atomic_load((gu64*)(ws + off0), RLX_AGENT), b = __hip_atomic_load((gu64*)(ws + off1), RLX_AGENT);
    const u64 m = __ballot(a == tag && b == tag);
    asm volatile("" ::: "memory");
    return m;
}

template <int S0, int NS, int NG, int NR, int RPG, int DR, bool LASTBAR, class RD, class MM>
DI void gemm_loop(RD rd, MM mm) {
    unsigned so = (S0 & 3) * RSLOT;
#pragma unroll
    for (int i = 0; i < NR; ++i) rd(so, 0, i);
    auto half = [&](int kk) {
        SB();
#pragma unroll
        for (int gq = 0; gq < NG; ++gq) {
            mm(kk, gq); SB();
#pragma unroll
            for (int i = gq * RPG; i < (gq + 1) * RPG && i < NR; ++i) rd(so, kk ^ 1, i);
            SB();
        }
        __builtin_amdgcn_s_waitcnt(0xC07F);
    };
    __builtin_amdgcn_s_waitcnt(0xC07F);
    _Pragma("unroll 1") for (int it = 0; it < NS - 1; ++it) {
        half(0);
        if (it == DR - 1) WAIT_VM(0);
        asm volatile("s_barrier" ::: "memory");
        so = so == 3 * RSLOT ? 0 : so + RSLOT;
        half(1);
    }
    half(0);
    if (LASTBAR) asm volatile("s_barrier" ::: "memory");
#pragma unroll
    for (int gq = 0; gq < NG; ++gq) mm(1, gq);
}

DI void ab_compute(bool isb, int m0, int n0, rsrc_t rws, unsigned lds0, int wv) {
    const int l = threadIdx.x & 63, i16 = l & 15, g = l >> 4;
    const unsigned xrd = lds0 + (wv * 32 + i16) * 128 + ((g ^ (i16 & 7)) << 4), wrd = lds0 + 16384 + i16 * 128 + ((g ^ (i16 & 7)) << 4);
    f32x4 acc[2][5];
#pragma unroll
    for (int j = 0; j < 5; ++j) { acc[0][j] = f32x4{0.f, 0.f, 0.f, 0.f}; acc[1][j] = f32x4{0.f, 0.f, 0.f, 0.f}; }
    bf16x8 xf[2][2], wf[2][5];
    unsigned xa, wa;
    gemm_loop<10, 10, 5, 7, AB_RPG, QKV_DR, false>(
        [&](unsigned so, int kk, int i) {
            if (i == 0) { xa = (xrd ^ (kk << 6)) + so; PIN(xa); }
            if (i == 2) { wa = (wrd ^ (kk << 6)) + so; PIN(wa); }
            if (i < 2) xf[kk][i] = lds_rd16(xa + i * 2048);
            else wf[kk][i - 2] = lds_rd16(wa + (i - 2) * 2048);
        },
        [&](int kk, int j) {
            acc[0][j] = MFMA16(xf[kk][0], wf[kk][j], acc[0][j]);
            acc[1][j] = MFMA16(xf[kk][1], wf[kk][j], acc[1][j]);
        });
    const float sg = isb ? -1.f : 1.f;
    const int ntile = n0 / 160, e16 = (n0 - ntile * 160) >> 4, k32 = ((isb ? 2048 : 0) + m0 + 32 * wv) >> 5;
#pragma unroll
    for (int j = 0; j < 5; ++j)
        st16(rws, OFF_AB + (unsigned)(((ntile * 128 + k32) * 10 + e16 + j) * 1024 + l * 16), pack8(acc[0][j] * sg, acc[1][j] * sg));
}

template <int KIND>
DI void qkv_compute(int m0, int nt, rsrc_t rws, unsigned lds0, int wv) {
    const int l = threadIdx.x & 63, wm = wv & 1, wn = wv >> 1, r32 = l & 31, hh = l >> 5, q = (l >> 2) & 3, p = l & 3;
    const unsigned xrd = lds0 + (wm * 64 + r32) * 128 + ((hh ^ ((r32 >> 1) & 7)) << 4);
    const int pq = KIND == 1 ? ((p & 1) << 1) | (p >> 1) : p;
    const unsigned wrd = lds0 + 16384 + (8 * hh + q) * 256 + (((2 * wn) ^ q) << 6) + ((l >> 4) & 1) * 32 + 8 * pq;
    const bool isq = nt < 5;
    unsigned off[2][2][2];
#pragma unroll
    for (int j = 0; j < 2; ++j)
#pragma unroll
        for (int s2 = 0; s2 < 2; ++s2) {
            if (KIND == 2) {
                const int col = (nt - 10) * 128 + wn * 64 + 32 * j + 16 * s2 + (l & 15), s = m0 + wm * 64;
                const int h = col / 20, d = col - h * 20;
                const int vp = d < 8 ? d : (d < 18 ? d - 8 : d - 10), vh = (d >= 4 && d < 12) || d == 16 || d == 17;
                off[j][s2][0] = nt >= 15 ? OOB : OFF_KV + (unsigned)(h * KV_HEAD + 8192 + vp * 1024 + vh * 512 + ((l >> 4) & 1) * 32 + hh * 16 + (s >> 8) * KV_CHUNK + (s & 255) * 2);
                off[j][s2][1] = off[j][s2][0] + 64;
            } else {
                const int pos = isq ? swap23(r32) : r32;
                const int c = (isq ? nt : nt - 5) * 128 + wn * 64 + 32 * j + 16 * s2 + 8 * hh;
                const int pair = c / 40, oc = (c - pair * 40) >> 3;
                const int h = 2 * pair + (oc > 2), half = oc > 2 ? oc - 3 : oc, tile = (m0 + wm * 64) >> 5;
                const unsigned o2 = (isq ? OFF_Q2 : OFF_K2) + pair * 32768 + (tile * 32 + pos) * 16;
                const unsigned o1 = (isq ? OFF_Q1 + h * 65536 + tile * 1024 : OFF_KV + h * KV_HEAD + (tile >> 3) * KV_CHUNK + (tile & 7) * 1024) + (half * 32 + pos) * 16;
                off[j][s2][0] = oc == 2 ? o2 : o1;
                off[j][s2][1] = oc == 2 ? o2 + 512 : o1 + 1024;
            }
            PIN(off[j][s2][0]); PIN(off[j][s2][1]);
        }
    f32x16 acc[2][2];
#pragma unroll
    for (int i = 0; i < 16; ++i) { acc[0][0][i] = 0.f; acc[0][1][i] = 0.f; acc[1][0][i] = 0.f; acc[1][1][i] = 0.f; }
    bf16x8 xf[2][2][2], wf[2][2][2];
    unsigned xa[2], wa[2];
    asm volatile("s_barrier" ::: "memory");
    gemm_loop<0, 10, 8, 8, 2, 0, true>(
        [&](unsigned so, int kk, int i) {
            const int k2 = i >> 2, j = i & 1;
            if (i == 0) { xa[0] = (xrd ^ (kk << 6)) + so; xa[1] = xa[0] ^ 32; wa[0] = wrd + so; wa[1] = wa[0] ^ 64; PIN(xa[0]); PIN(xa[1]); PIN(wa[0]); PIN(wa[1]); }
            if (i & 2) { const unsigned a = wa[j] + (kk * 2 + k2) * 4096; wf[kk][k2][j] = lds_tr2(a, a + 1024); }
            else xf[kk][k2][j] = lds_rd16(xa[k2] + j * 4096);
        },
        [&](int kk, int gq) {
            const int k2 = gq >> 2, m = (gq >> 1) & 1, j = gq & 1;
            acc[m][j] = (KIND == 1) ? MFMA32(wf[kk][k2][j], xf[kk][k2][m], acc[m][j]) : MFMA32(xf[kk][k2][m], wf[kk][k2][j], acc[m][j]);
        });
    const float sc = isq ? 0.22360679774997896f * 1.4426950408889634f : 1.f;
#pragma unroll
    for (int m = 0; m < 2; ++m)
#pragma unroll
        for (int j = 0; j < 2; ++j) {
            const f32x16& a = acc[m][j];
            u32x4 lo, hi;
#pragma unroll
            for (int i = 0; i < 4; ++i) {
                if (KIND == 2) {
                    const auto r = __builtin_amdgcn_permlane16_swap(pk(a[2 * i], a[2 * i + 1]), pk(a[8 + 2 * i], a[9 + 2 * i]), false, false);
                    lo[i] = r[0]; hi[i] = r[1];
                } else {
                    lo[i] = pk(a[2 * i] * sc, a[2 * i + 1] * sc); hi[i] = pk(a[8 + 2 * i] * sc, a[9 + 2 * i] * sc);
                }
            }
            st16(rws, off[j][0][m], lo);
            st16(rws, off[j][1][m], hi);
        }
}

DI float half_swap_max(float m) {
    auto r = __builtin_amdgcn_permlane32_swap(__float_as_uint(m), __float_as_uint(m), false, false);
    return fmaxf(__uint_as_float(r[0]), __uint_as_float(r[1]));
}
DI float max16(const f32x16& s) {
    float a = fmaxf(fmaxf(s[0], s[1]), s[2]), b = fmaxf(fmaxf(s[3], s[4]), s[5]);
    a = fmaxf(fmaxf(a, s[6]), s[7]); b = fmaxf(fmaxf(b, s[8]), s[9]);
    a = fmaxf(fmaxf(a, s[10]), s[11]); b = fmaxf(fmaxf(b, s[12]), s[13]);
    a = fmaxf(fmaxf(a, s[14]), s[15]);
    return fmaxf(a, b);
}

__global__ __launch_bounds__(512, 2) void fused_kernel(const bf16* x, const bf16* cr, const bf16* ci, const bf16* wr, const bf16* wi,
                                                       const bf16* wqkv, const bf16* wo, const bf16* gw, char* ws, bf16* out) {
    extern __shared__ __attribute__((aligned(16))) char smem[];
    const unsigned lds0 = (unsigned)(uintptr_t)(LDSP char*)smem;
    const int b = blockIdx.x, xcd = b & 7, slot = b >> 3, l = threadIdx.x & 63;
    const int wv = __builtin_amdgcn_readfirstlane(threadIdx.x >> 6);
    const int mt1 = (xcd & 1) * 8 + (slot & 7), nt1 = (xcd >> 1) * 4 + (slot >> 3);
    const int head = xcd * 4 + (slot >> 3), qb = slot & 7, pair = head >> 1, odd = head & 1;
    const int kq = xcd & 3, mt = (xcd >> 2) * 8 + (slot >> 2), ntile = slot & 3;
    const int mt3 = xcd * 4 + (slot >> 3), nt3 = slot & 7;
    const rsrc_t rws = mk_rsrc(ws, WS_BYTES);
    const unsigned lane16 = l * 16;
    const int hh = l >> 5, r32 = l & 31, i16 = l & 15, g = l >> 4;
    const int qg = wv & 3, kh = wv >> 2;
    const unsigned swz = (unsigned)(((l & 7) ^ (l >> 3)) << 4);
    u64 tag;
    bool kv_all = true;
    const unsigned kvsrc = OFF_KV + head * KV_HEAD + qg * 1024, k2src = OFF_K2 + pair * 32768 + qg * 1024;
    auto kv_piece = [&](int i, int c, int sl) {
        const unsigned dst = lds0 + sl * AT_SLOT, src = kvsrc + c * KV_CHUNK;
        if (i < 2) dma16c(dst + qg * 1024 + i * 4096, lane16, rws, src + i * 4096);
        else if (i == 2) dma16c(dst + 8192 + qg * 1024, lane16, rws, k2src + c * 4096);
        else if (i < 5 || qg < 2) dma16c(dst + 12288 + (qg + (i - 3) * 4) * 1040, lane16, rws, src + 8192 + (i - 3) * 4096);
    };
    unsigned voff, koff, k2off, coff, tch_c, tch_w, tch_a, kvv2;
    auto setup = [&]() {
        const bool vdata = r32 < 18 || r32 == 20 || r32 == 21, vone = r32 == 18 || r32 == 22;
        const int vp = r32 < 4 ? r32 : (r32 < 12 ? r32 - 4 : (r32 < 18 ? r32 - 8 : r32 - 12)), vh = (r32 >= 4 && r32 < 12) || r32 == 16 || r32 == 17;
        voff = 12288 + hh * 16 + kh * 256 + (vdata ? vp * 1040 + vh * 512 : (vone ? 10400 : 10928));
        koff = lane16 + kh * 4096; k2off = 8192 + lane16 + kh * 2048;
        coff = (unsigned)((mt * 128 + 32 * qg + (l >> 3)) * 4096 + (kq & 1) * 2048) + swz;
        tch_c = l < 16 ? (unsigned)((mt * 128 + ntile * 32 + wv * 4) * 4096 + (kq & 1) * 2048 + l * 128) : OOB;
        tch_w = l < 25 ? (unsigned)(((slot * 8 + wv) * 25 + l) * 128) : OOB;
        tch_a = l < 40 ? OFF_AB + (unsigned)((ntile * 64 + kq * 16) * 20480 + (((mt & 7) * 8 + wv) * 40 + l) * 128) : OOB;
        kvv2 = wv < 6 ? lane16 : OOB;
        PIN(voff); PIN(koff); PIN(k2off); PIN(coff); PIN(tch_c); PIN(tch_w); PIN(tch_a); PIN(kvv2);
    };
    int tl_n = 1;
    const char* pc;
    asm volatile("s_getpc_b64 %0" : "=s"(pc));

    if (__builtin_expect(kh, 1)) {
        __builtin_amdgcn_s_setprio(L_PRIO);
        const rsrc_t rx = mk_rsrc(x, S_ * D_ * 2), rw = mk_rsrc(wqkv, D_ * 1920 * 2);
        const unsigned xoff = (unsigned)((mt1 * 128 + 32 * qg + 8 * hh + ((l >> 3) & 3)) * (D_ * 2)) + swz;
        const int k0 = QKV_ROT == 5 ? (2 * (slot & 7) + (((slot >> 3) * 5 + 1) >> 1)) % 10 : QKV_ROT == 2 ? 5 * ((slot ^ (slot >> 3)) & 1) : 0;
        const unsigned xoffq = (unsigned)((mt1 * 128 + 64 * (qg >> 1) + 4 * (qg & 1) + 8 * hh + ((l >> 3) & 3)) * (D_ * 2) + (((l & 7) ^ (4 * (qg & 1) + g)) << 4));
        const unsigned woff = (unsigned)((16 * qg + g) * 3840 + (((i16 >> 2) ^ g) << 6) + ((i16 & 3) << 4) + (nt1 < 15 ? nt1 : 14) * 256);
        auto x_part = [&](int ks, unsigned dst) {
#pragma unroll
            for (int i = 0; i < 4; ++i) dma16(dst + (qg * 4 + i) * 1024, xoff, rx, ks * 128 + (16 * (i & 1) + 4 * (i >> 1)) * (D_ * 2));
        };
        auto issue_qkv = [&](int k) {
            const int ks = k0 + k - (k0 + k >= 10 ? 10 : 0);
            const unsigned dst = lds0 + (k & 3) * RSLOT;
#pragma unroll
            for (int i = 0; i < 4; ++i) dma16(dst + (8 * (qg >> 1) + (qg & 1) + 2 * i) * 1024, xoffq, rx, ks * 128 + 16 * i * (D_ * 2));
#pragma unroll
            for (int i = 0; i < 4; ++i) dma16(dst + 16384 + (qg * 4 + i) * 1024, woff, rw, (ks * 64 + 4 * i) * 3840);
        };
        if (qg == 0) touch(lds0 + TOUCH_LDS, l < 9 ? (unsigned)((l * 32 + slot) * 128) : OOB, mk_rsrc(pc, CODE_PF));
        issue_qkv(0); issue_qkv(1);
        tag = epoch_tag(ws);
        VM_BAR(8); issue_qkv(2); issue_qkv(3);
        const rsrc_t rd = mk_rsrc(nt1 >= 8 ? wi : wr, D_ * D_ * 2);
        const unsigned doff = (unsigned)(((nt1 & 7) * 80 + 24 * qg + (l >> 3)) * (D_ * 2)) + swz;
        const int n0 = (pair * 40) >> 7, n1 = (pair * 40 + 39) >> 7, sec = (l >> 2) & 3, sb5 = sec == 3 ? 0 : sec * 5;
        const int frow = hh ? (5 + (g & 1) * 5) * 16 + i16 : sb5 * 16 + ((qb * 2 + (sec == 1 || sec == 2 ? (l & 3) : (l & 1))) & 15);
        const unsigned fq0 = OFF_FQ + (n0 * 16 + frow) * 64, fq1 = OFF_FQ + (n1 * 16 + frow) * 64, look = lds0 + 2 * RSLOT + 34816;
        unsigned dv1 = qg == 3 ? OOB : doff, dv2 = dv1;
        const rsrc_t rd2 = mk_rsrc(qg == 3 ? (const void*)ws : (const void*)(nt1 >= 8 ? wi : wr), qg == 3 ? +WS_BYTES : D_ * D_ * 2u);
        auto issue_ab = [&](int k) {
            const unsigned dst = lds0 + ((k + 2) & 3) * RSLOT;
            x_part(k, dst);
            dma16(dst + 16384 + qg * 3072, doff, rd, k * 128);
            dma16c(qg == 3 ? look : dst + 16384 + qg * 3072 + 1024, dv1, rd2, qg == 3 ? 0 : k * 128 + 8 * (D_ * 2));
            dma16c(qg == 3 ? look + 1024 : dst + 16384 + qg * 3072 + 2048, dv2, rd2, qg == 3 ? 0 : k * 128 + 16 * (D_ * 2));
        };
        auto seen_lds = [&]() { return tag ? __ballot(*(volatile LDSP u64*)(look + lane16) == tag && *(volatile LDSP u64*)(look + 1024 + lane16) == tag) : 0ull; };
        touch(lds0 + TOUCH_LDS, l < 25 ? (unsigned)((((xcd >> 1) & 1) * 3200 + (slot * 4 + qg) * 25 + l) * 128) : OOB, rd);
        VM_BAR(17); issue_qkv(4);
        VM_BAR(17); issue_qkv(5);
        _Pragma("unroll 1") for (int s = 3; s < 7; ++s) { VM_BAR(16); issue_qkv(s + 3); }
        VM_BAR(16); issue_ab(0);
        VM_BAR(15); issue_ab(1);
        VM_BAR(14); issue_ab(2);
        VM_BAR(14); issue_ab(3);
        {
            const int lg = lane_here();
            const rsrc_t rg = mk_rsrc(gw, D_ * 2);
#pragma unroll
            for (int i = 0; i < 3; ++i) dma16(lds0 + (2 + (qg >> 1)) * RSLOT + 28672 + (qg & 1) * 3072 + i * 1024, i < 2 || lg < 32 ? lane16 : OOB, rx, (b * 8 + qg * 2) * (D_ * 2) + i * 1024);
            dma16(lds0 + TOUCH_LDS + 1024, lane16, rg, 0);
            dma16(lds0 + TOUCH_LDS + 2048, lane16 + 1024, rg, 0);
        }
        setup();
        _Pragma("unroll 1") for (int s = 11; s < 17; ++s) {
            VM_BAR(14);
            if (s == 10 + QKV_DR && qg == 0) set_flag(ws, OFF_FQ + (nt1 * 16 + mt1) * 64, tag);
            if (s == 16 && qg == 3) { dv1 = fq0; dv2 = fq1; }
            issue_ab(s - 7);
        }
        VM_BAR(14);
        VM_BAR(7);
        u64 seen = seen_lds();
        const bool early = (unsigned)seen == 0xffffffffu;
        if (!early) {
            VM_BAR(0); seen = seen_lds();
            _Pragma("unroll 1") for (unsigned spins = 0; spins < SPIN_CAP && (unsigned)seen != 0xffffffffu; ++spins) {
                seen = look2(ws, fq0, fq1, tag);
                __builtin_amdgcn_s_sleep(POLL_SLEEP);
            }
        }
        STAMP(ws, 2);
        dma16c(lds0 + Q_LDS + qg * 2048, lane16, rws, OFF_Q1 + head * 65536 + (qb * 8 + qg * 2) * 1024);
        dma16c(lds0 + Q_LDS + qg * 2048 + 1024, lane16, rws, OFF_Q1 + head * 65536 + (qb * 8 + qg * 2 + 1) * 1024);
        dma16c(lds0 + Q_LDS + 8192 + qg * 1024, lane16, rws, OFF_Q2 + pair * 32768 + (qb * 4 + qg) * 1024);
#pragma unroll
        for (int i = 0; i < 6; ++i) kv_piece(i, qb, 0);
        if (early) VM_BAR(8);
#pragma unroll
        for (int i = 0; i < 6; ++i) kv_piece(i, (qb + 1) & 7, 1);
        kv_all = seen == ~0ull;
        if (qg < 3) {
            const unsigned c = hh ? 0u : 0x3F803F80u;
            *(LDSP u32x4*)(lds0 + qg * AT_SLOT + 12288 + 10400 + lane16 + hh * 16) = u32x4{c, c, c, c};
        }
        float gs = 0.f;
#pragma unroll
        for (int i = 0; i < 3; ++i) {
            const unsigned c = i < 2 || r32 < 16 ? (r32 + 32 * i) * 16 : 0;
            const u32x4 gx = __builtin_bit_cast(u32x4, lds_rd16(lds0 + (2 + (qg >> 1)) * RSLOT + 28672 + (qg & 1) * 3072 + hh * 1280 + c));
            const u32x4 gg = __builtin_bit_cast(u32x4, lds_rd16(lds0 + TOUCH_LDS + 1024 + c));
            float t = 0.f;
#pragma unroll
            for (int j = 0; j < 4; ++j) t += bf_lo(gx[j]) * bf_lo(gg[j]) + bf_hi(gx[j]) * bf_hi(gg[j]);
            gs += i < 2 || r32 < 16 ? t : 0.f;
        }
#pragma unroll
        for (int off = 16; off > 0; off >>= 1) gs += __shfl_xor(gs, off);
        if (r32 < 16) st4(rws, OFF_G + (b * 8 + qg * 2 + hh) * 64 + r32 * 4, __float_as_uint(1.f / (1.f + __expf(-gs))));
        __builtin_amdgcn_s_setprio(0);
        asm volatile("s_waitcnt vmcnt(6) lgkmcnt(0)\n\ts_barrier" ::: "memory");
    } else {
        tag = epoch_tag(ws);
        setup();
        STAMP(ws, 1);
        if (nt1 < 10) qkv_compute<1>(mt1 * 128, nt1, rws, lds0, wv);
        else qkv_compute<2>(mt1 * 128, nt1, rws, lds0, wv);
        STAMP(ws, 2);
        ab_compute(nt1 >= 8, mt1 * 128, (nt1 & 7) * 80, rws, lds0, wv);
        STAMP(ws, 3);
        asm volatile("s_barrier" ::: "memory");
    }
    TL_LOG();
    STAMP(ws, 4);

    const bf16* cmat = kq < 2 ? cr : ci;
    const rsrc_t rc = mk_rsrc(cmat, S_ * S_ * 2), rwo = mk_rsrc(wo, D_ * D_ * 2);
    const unsigned absrc = OFF_AB + (unsigned)((ntile * 64 + kq * 16) * 20480 + qg * 5120);
    auto dft_piece = [&](int i, int k) {
        const unsigned dst = lds0 + ((k + 2) & 3) * RSLOT;
        if (i < 5) dma16c(dst + 16384 + (qg * 5 + i) * 1024, lane16, rws, absrc + k * 20480 + i * 1024);
        else dma16(dst + (qg * 4 + i - 5) * 1024, coff, rc, k * 128 + (i - 5) * 8 * 4096);
    };
    bf16x8 q1[2], q2[2][2];
#pragma unroll
    for (int X = 0; X < 2; ++X) {
        q1[X] = lds_rd16(lds0 + Q_LDS + (qg * 2 + X) * 1024 + lane16);
        const u32x4 qr = __builtin_bit_cast(u32x4, lds_rd16(lds0 + Q_LDS + 8192 + ((qg * 2 + X) * 32 + r32) * 16));
#pragma unroll
        for (int P = 0; P < 2; ++P) {
            const bool lo = hh == P && !odd, hi = hh == P && odd;
            q2[X][P] = __builtin_bit_cast(bf16x8, u32x4{lo ? qr[0] : 0u, lo ? qr[1] : 0u, hi ? qr[2] : 0u, hi ? qr[3] : 0u});
        }
    }
    const f32x16 zero16 = {0.f, 0.f, 0.f, 0.f, 0.f, 0.f, 0.f, 0.f, 0.f, 0.f, 0.f, 0.f, 0.f, 0.f, 0.f, 0.f};
    unsigned sb = lds0;
    bf16x8 k1f[2], k2f, v0f, v1f;
    k1f[0] = lds_rd16(sb + koff); k2f = lds_rd16(sb + k2off); k1f[1] = lds_rd16(sb + koff + 1024);
    v0f = lds_rd16(sb + voff); v1f = v0f;
    f32x16 sc, negm[2], o[2];
    float m0[2];
    {
        f32x16 sB = MFMA32(k1f[0], q1[1], zero16);
        sB = MFMA32(k2f, q2[1][0], sB);
        sc = MFMA32(k1f[0], q1[0], zero16);
        sc = MFMA32(k2f, q2[0][0], sc);
        m0[1] = half_swap_max(max16(sB));
        m0[0] = half_swap_max(max16(sc));
#pragma unroll
        for (int i = 0; i < 16; ++i) { negm[0][i] = -m0[0]; negm[1][i] = -m0[1]; sc[i] -= m0[0]; }
    }
    o[0] = zero16; o[1] = zero16;
    u32x4 p0, p1 = {0u, 0u, 0u, 0u};
    float e12 = 0.f, e13 = 0.f, e14 = 0.f, e15 = 0.f;
    const unsigned kvs0 = OFF_KV + head * KV_HEAD + wv * 1024;
    const unsigned kvs2 = wv < 2 ? kvs0 + 16384 : OFF_K2 + pair * 32768 + (wv - 2) * 1024, kvstep2 = wv < 2 ? KV_CHUNK : 4096;
    const unsigned kvd2 = wv < 2 ? 12288 + (8 + wv) * 1040 : 8192 + ((wv - 2) & 3) * 1024;
    const int rare = wv == 4 ? 0x32 | (kv_all ? 0 : 1) | (b == 0 ? 8 : 0) : 0;
    int sl = 0;
    if (PRIO_TRICK) __builtin_amdgcn_s_setprio(1);
    STAMP(ws, 11);
#pragma unroll
    for (int last = 0; last < 2; ++last)
    _Pragma("unroll 1") for (int c = last ? 6 : 0; c < (last ? 8 : 6); ++c) {
        const int nsl = sl == 2 ? 0 : sl + 1;
        const unsigned sbn = lds0 + nsl * AT_SLOT;
#pragma unroll
        for (int u = 0; u < 8; ++u) {
            const int T = u >> 1, X = u & 1, Tn = (u + 1) >> 1, Xn = X ^ 1;
            if (u == 4) {
                if (!last && ((rare >> c) & 1)) {
                    const int lh = lane_here();
                    if (c == 0) {
                        const int s5 = 5 + ((lh >> 4) & 1) * 5;
                        poll2(ws, OFF_FQ + ((s5 + ((pair * 40) >> 7)) * 16 + (lh & 15)) * 64, OFF_FQ + ((s5 + ((pair * 40 + 39) >> 7)) * 16 + (lh & 15)) * 64, tag);
                    }
                    if (c == 3) {
                        poll2(ws, OFF_FQ + lh * 64, OFF_FQ + (64 + lh) * 64, tag);
                        poll2(ws, OFF_FQ + (128 + lh) * 64, OFF_FQ + (192 + lh) * 64, tag);
                        set_flag(ws, OFF_EPOCH, tag);
                    }
                    const unsigned fa = lh < 32 ? OFF_FAB + (((kq >> 1) * 8 + ntile * 2 + ((lh >> 3) & 1)) * 16 + (kq & 1) * 8 + (lh & 7)) * 64 : OFF_FG + (mt3 * 8 + (lh & 7)) * 64;
                    WAIT_VM(0);
                    if (c == 1) { set_flag(ws, OFF_FAB + (nt1 * 16 + mt1) * 64, tag); set_flag(ws, OFF_FG + b * 64, tag); }
                    if (c == 4) dma16c(lds0 + TOUCH_LDS + 1024, fa, rws, 0);
                    if (c == 5 && !__all(*(volatile LDSP u64*)(lds0 + TOUCH_LDS + 1024 + lh * 16) == tag)) poll2(ws, fa, fa, tag);
                } else WAIT_VM(0);
                VM_BAR(63);
                if (PRIO_TRICK) __builtin_amdgcn_s_setprio(1);
            }
            if (PRIO_TRICK && (u == PRIO_U || (last && u == 4 + PRIO_U && c == 7))) __builtin_amdgcn_s_setprio(0);
            float a0, a1, a2, a3, b0, b1, b2, b3;
            SB();
            f32x16 sn = MFMA32(k1f[Tn & 1], q1[Xn], negm[Xn]);
            if (X) k1f[(Tn + 1) & 1] = Tn + 1 < 4 ? lds_rd16(sb + koff + (Tn + 1) * 1024) : lds_rd16(sbn + koff + (Tn - 3) * 1024);
            p1[2] = pk(e12, e13); p1[3] = pk(e14, e15); PIN(p1);
            a0 = EX2(sc[0]); a1 = EX2(sc[1]); a2 = EX2(sc[2]); a3 = EX2(sc[3]); PIN(a0); PIN(a1); PIN(a2); PIN(a3);
            SB();
            sn = MFMA32(k2f, q2[Xn][Tn & 1], sn);
            if (u == 2) k2f = lds_rd16(sb + k2off + 1024);
            if (u == 6) k2f = lds_rd16(sbn + k2off);
            p0[0] = pk(a0, a1); p0[1] = pk(a2, a3);
            b0 = EX2(sc[4]); b1 = EX2(sc[5]); b2 = EX2(sc[6]); b3 = EX2(sc[7]); PIN(b0); PIN(b1); PIN(b2); PIN(b3);
            SB();
            if (last && u == 4 && !kh) dft_piece(4, c - 6);
            o[Xn] = MFMA32(v1f, __builtin_bit_cast(bf16x8, p1), o[Xn]);
            if (!X) v1f = lds_rd16(sb + voff + T * 64 + 32);
            p0[2] = pk(b0, b1); p0[3] = pk(b2, b3); PIN(p0);
            a0 = EX2(sc[8]); a1 = EX2(sc[9]); a2 = EX2(sc[10]); a3 = EX2(sc[11]); PIN(a0); PIN(a1); PIN(a2); PIN(a3);
            SB();
            o[X] = MFMA32(v0f, __builtin_bit_cast(bf16x8, p0), o[X]);
            if (X) v0f = T < 3 ? lds_rd16(sb + voff + (T + 1) * 64) : lds_rd16(sbn + voff);
            e12 = EX2(sc[12]); e13 = EX2(sc[13]); e14 = EX2(sc[14]); e15 = EX2(sc[15]); PIN(e12); PIN(e13); PIN(e14); PIN(e15);
            SB();
            p1[0] = pk(a0, a1); p1[1] = pk(a2, a3);
            SB();
            if (!last) {
                if (u >= 4) {
                    int cu = c, su = sl;
                    asm volatile("" : "+s"(cu), "+s"(su));
                    const int cn = (qb + cu + 2) & 7;
                    const unsigned sbl = lds0 + (su == 0 ? 2 : su - 1) * AT_SLOT;
                    if (u == 4) dma16c(sbl + wv * 1024, lane16, rws, kvs0 + cn * KV_CHUNK);
                    if (u == 5) dma16c(sbl + 12288 + wv * 1040, lane16, rws, kvs0 + cn * KV_CHUNK + 8192);
                    if (u == 6) dma16c(wv < 6 ? sbl + kvd2 : lds0 + TOUCH_LDS, kvv2, rws, kvs2 + cn * kvstep2);
                    if (u == 7) touch(lds0 + TOUCH_LDS, cu < 4 ? tch_c + cu * 4096 : (cu == 4 ? +tch_w : +tch_a),
                                      mk_rsrc(cu < 4 ? (const void*)cmat : (cu == 4 ? (const void*)wo : (const void*)ws),
                                              cu < 4 ? S_ * S_ * 2u : (cu == 4 ? D_ * D_ * 2u : +WS_BYTES)));
                }
            } else if (u >= 4) {
                const unsigned dst = lds0 + (c - 4) * RSLOT;
                const int i = kh * 2 + (u & 1);
                if (u < 6) dma16c(dst + 16384 + (qg * 5 + i) * 1024, lane16, rws, absrc + (c - 6) * 20480 + i * 1024);
                else dma16(dst + (qg * 4 + i) * 1024, coff, rc, (c - 6) * 128 + i * 8 * 4096);
            }
            sc = sn;
        }
        sl = nsl; sb = sbn;
    }
    p1[2] = pk(e12, e13); p1[3] = pk(e14, e15);
    o[1] = MFMA32(v1f, __builtin_bit_cast(bf16x8, p1), o[1]);
    STAMP(ws, 5);
    {
        f32x16 of;
        const f32x16 ox = kh ? o[0] : o[1], om = kh ? o[1] : o[0];
        const float mx = kh ? m0[0] : m0[1], mm = kh ? m0[1] : m0[0];
        const unsigned xw = lds0 + 2 * AT_SLOT + wv * 3072 + lane16, xr = lds0 + 2 * AT_SLOT + (wv ^ 4) * 3072 + lane16;
        *(LDSP f32x4*)(xw) = f32x4{ox[0], ox[1], ox[2], ox[3]};
        *(LDSP f32x4*)(xw + 1024) = f32x4{ox[4], ox[5], ox[6], ox[7]};
        *(LDSP f32x4*)(xw + 2048) = f32x4{ox[8], ox[9], ox[10], mx};
        ALL_BAR(); TL_LOG();
        if (kh) {
#pragma unroll
            for (int i = 0; i < 9; ++i) dft_piece(i, 2);
        }
        const f32x4 r0 = *(const LDSP f32x4*)(xr), r1 = *(const LDSP f32x4*)(xr + 1024), r2 = *(const LDSP f32x4*)(xr + 2048);
        const float mn = fmaxf(mm, r2[3]), fa = EX2(mm - mn), fb = EX2(r2[3] - mn);
#pragma unroll
        for (int i = 0; i < 4; ++i) { of[i] = om[i] * fa + r0[i] * fb; of[4 + i] = om[4 + i] * fa + r1[i] * fb; }
#pragma unroll
        for (int i = 0; i < 3; ++i) of[8 + i] = om[8 + i] * fa + r2[i] * fb;
        float asum = 0.f;
#pragma unroll
        for (int i = 0; i < 10; ++i) asum += fabsf(of[i]);
        const bool bad = !(om[10] <= 1.0e18f) || !(r2[2] <= 1.0e18f) || !(asum <= 1.0e37f);
        if (__builtin_expect(__any(bad), 0)) {
            const bf16x8 fq1 = kh ? q1[1] : q1[0], fq2 = kh ? q2[1][0] : q2[0][0];
            const int rr = lane_here() & 31;
            const bool vdata = rr < 18 || rr == 20 || rr == 21, vone = rr == 18 || rr == 22;
            const int vp = rr < 4 ? rr : (rr < 12 ? rr - 4 : (rr < 18 ? rr - 8 : rr - 12)), vh = (rr >= 4 && rr < 12) || rr == 16 || rr == 17;
            const unsigned vg = 8192 + hh * 16 + (vdata ? vp * 1024 + vh * 512 : 0), vc = vone ? 0x3F803F80u : 0u;
            const u32x4 vconst = {vc, vc, vc, vc};
            float m = -INFINITY;
            of = zero16;
            _Pragma("unroll 1") for (int t = 0; t < 64; ++t) {
                const unsigned cb = OFF_KV + head * KV_HEAD + (t >> 3) * KV_CHUNK;
                const int T = t & 7;
                f32x16 s = MFMA32(__builtin_bit_cast(bf16x8, ld16(rws, cb + T * 1024 + lane16)), fq1, zero16);
                s = MFMA32(__builtin_bit_cast(bf16x8, ld16(rws, OFF_K2 + pair * 32768 + (t * 32 + r32) * 16)), fq2, s);
                const float mq = fmaxf(m, half_swap_max(max16(s)));
                const float al = EX2(m - mq);
                m = mq;
#pragma unroll
                for (int i = 0; i < 16; ++i) { of[i] *= al; s[i] = EX2(s[i] - mq); }
                const u32x4 pa = {pk(s[0], s[1]), pk(s[2], s[3]), pk(s[4], s[5]), pk(s[6], s[7])};
                const u32x4 pb = {pk(s[8], s[9]), pk(s[10], s[11]), pk(s[12], s[13]), pk(s[14], s[15])};
                const u32x4 va = ld16(rws, cb + vg + T * 64), vb2 = ld16(rws, cb + vg + T * 64 + 32);
                of = MFMA32(__builtin_bit_cast(bf16x8, vdata ? va : vconst), __builtin_bit_cast(bf16x8, pa), of);
                of = MFMA32(__builtin_bit_cast(bf16x8, vdata ? vb2 : vconst), __builtin_bit_cast(bf16x8, pb), of);
            }
        }
        const float rl = 1.f / of[10];
        const int q = (qb * 8 + qg * 2 + kh) * 32 + r32;
        st16(rws, OFF_OM + ((q >> 4) * 16 + pair) * 1024 + (odd * 2 + hh) * 256 + (q & 15) * 16,
             u32x4{pk(of[0] * rl, of[1] * rl), pk(of[2] * rl, of[3] * rl), pk(of[4] * rl, of[5] * rl), pk(of[6] * rl, of[7] * rl)});
        const unsigned xl = pk(of[8] * rl, of[9] * rl);
        const auto rx = __builtin_amdgcn_permlane32_swap(xl, xl, false, false);
        if (!hh) st8(rws, OFF_OL + (head * S_ + q) * 8, u32x2{rx[0], rx[1]});
    }
    STAMP(ws, 6);

    const int wt = qg & 1, we = qg >> 1;
    const unsigned zx = lds0 + Q_LDS + qg * 4096 + lane16;
    const unsigned zx2 = lds0 + 3 * RSLOT + qg * 2048 + lane16;
    if (kh) {
        __builtin_amdgcn_s_setprio(L_PRIO);
        auto issue_dft = [&](int k) {
#pragma unroll
            for (int i = 0; i < 9; ++i) dft_piece(i, k);
        };
        LGKM_BAR(); TL_LOG(); issue_dft(3); issue_dft(4);
        unsigned wo1[5], wo2[5];
#pragma unroll
        for (int i = 0; i < 5; ++i) {
            const int cc = i * 64 + lane_here(), rho = cc / 10, ka = swap23(rho), col = (cc - rho * 10) * 16 + nt3 * 160;
            wo1[i] = (unsigned)((40 * qg + 20 * (ka >> 4) + (ka & 15)) * (D_ * 2) + col);
            wo2[i] = (unsigned)((160 * qg + 20 * (ka >> 2) + 16 + (ka & 3)) * (D_ * 2) + col);
        }
        const unsigned fo = OFF_FO + ((mt3 >> 2) * 32 + r32) * 64;
        _Pragma("unroll 1") for (int s = 2; s < 13; ++s) {
            VM_BAR(18);
            if (s == 3 && qg == 0) set_flag(ws, OFF_FO + (qb * 32 + head) * 64, tag);
            if (qg == 1) {
                if (s == 10) dma16c(lds0 + TOUCH_LDS, fo, rws, 0);
                if (s == 12 && !__all(*(volatile LDSP u64*)(lds0 + TOUCH_LDS + lane16) == tag)) poll2(ws, fo, fo, tag);
            }
            issue_dft(s + 3);
        }
        _Pragma("unroll 1") for (int j = 0; j < 4; ++j) {
            VM_BAR(18);
            if (j == 3) VM_BAR(0);
            const unsigned dst = lds0 + ((j + 2) & 3) * RSLOT;
#pragma unroll
            for (int i = 0; i < 4; ++i) dma16c(dst + (qg * 4 + i) * 1024, lane16, rws, OFF_OM + ((mt3 * 4 + i) * 16 + j * 4 + qg) * 1024);
#pragma unroll
            for (int i = 0; i < 5; ++i) dma16(dst + 16384 + qg * 5120 + i * 1024, wo1[i], rwo, j * 160 * (D_ * 2));
        }
#pragma unroll
        for (int i = 0; i < 4; ++i)
            dma16c(lds0 + 2 * RSLOT + (qg * 4 + i) * 1024, (unsigned)(hh * (S_ * 8) + r32 * 16), rws, OFF_OL + ((qg * 8 + i * 2) * S_ + mt3 * 64) * 8);
#pragma unroll
        for (int i = 0; i < 5; ++i) dma16(lds0 + 2 * RSLOT + 16384 + qg * 5120 + i * 1024, wo2[i], rwo, 0);
        const float gv = __uint_as_float(__builtin_amdgcn_raw_buffer_load_b32(rws, OFF_G + (mt3 * 64 + wt * 32 + r32) * 64, 0, 16));
        VM_BAR(0);
        { const unsigned fy = OFF_FY + ((mt3 * 8 + nt3) * 4 + (l & 3)) * 64; poll2(ws, fy, fy, tag); }
        STAMP(ws, 8);
        u32x4 yv[3][4];
#pragma unroll
        for (int e = 0; e < 3; ++e) {
            const unsigned yo = e == 2 && we ? OOB : OFF_Y + (unsigned)(((mt3 * 2 + wt) * 40 + nt3 * 5 + we * 3 + e) * 1024) + lane16;
#pragma unroll
            for (int p = 0; p < 4; ++p) yv[e][p] = ld16(rws, yo + p * (64 * 40 * 1024));
        }
        asm volatile("s_barrier" ::: "memory");
        TL_LOG();
        const float g1 = 1.f - gv;
#pragma unroll
        for (int e = 0; e < 3; ++e) {
            if (e == 2 && we) break;
            const unsigned za = e < 2 ? zx + e * 2048 : zx2;
            const f32x4 z0 = *(const LDSP f32x4*)(za), z1 = *(const LDSP f32x4*)(za + 1024);
            u32x4 v;
#pragma unroll
            for (int i = 0; i < 4; ++i) {
                const float y0 = (bf_lo(yv[e][0][i]) + bf_lo(yv[e][1][i])) + (bf_lo(yv[e][2][i]) + bf_lo(yv[e][3][i]));
                const float y1 = (bf_hi(yv[e][0][i]) + bf_hi(yv[e][1][i])) + (bf_hi(yv[e][2][i]) + bf_hi(yv[e][3][i]));
                const float za0 = i < 2 ? z0[2 * i] : z1[2 * i - 4], za1 = i < 2 ? z0[2 * i + 1] : z1[2 * i - 3];
                v[i] = pk(gv * y0 + g1 * za0, gv * y1 + g1 * za1);
            }
            *(u32x4*)((char*)out + (unsigned)((mt3 * 64 + wt * 32 + r32) * (D_ * 2) + (nt3 * 80 + (we * 3 + e) * 16 + hh * 8) * 2)) = v;
        }
        TL_LOG();
    } else {
        {
            const unsigned crd = lds0 + (wt * 64 + i16) * 128 + ((g ^ (i16 & 7)) << 4), ard = lds0 + 16384 + we * 5120 + lane16;
            f32x4 acc[5][4];
#pragma unroll
            for (int e = 0; e < 5; ++e)
#pragma unroll
                for (int t = 0; t < 4; ++t) acc[e][t] = f32x4{0.f, 0.f, 0.f, 0.f};
            bf16x8 cf[2][4], af[2][5];
            unsigned ca, aa;
            gemm_loop<2, 16, 10, 9, DFT_RPG, 3, true>(
                [&](unsigned so, int kk, int i) {
                    if (i == 0 && kk == 0) { aa = ard + so; PIN(aa); }
                    if (i == 1) { ca = (crd ^ (kk << 6)) + so; PIN(ca); }
                    if (i < 8 && (i & 1)) cf[kk][i >> 1] = lds_rd16(ca + (i >> 1) * 2048);
                    else af[kk][i >> 1] = lds_rd16(aa + kk * 10240 + (i >> 1) * 1024);
                },
                [&](int kk, int gq) {
                    const int e = gq >> 1;
#pragma unroll
                    for (int t = (gq & 1) * 2; t < (gq & 1) * 2 + 2; ++t) acc[e][t] = MFMA16(af[kk][e], cf[kk][t], acc[e][t]);
                });
            const unsigned ydst = OFF_Y + (unsigned)(((kq * 64 + mt * 4 + wt * 2) * 40 + ntile * 10 + we * 5) * 1024) + lane16;
#pragma unroll
            for (int e = 0; e < 5; ++e)
#pragma unroll
                for (int u = 0; u < 2; ++u) st16(rws, ydst + (u * 40 + e) * 1024, pack8x(acc[e][2 * u], acc[e][2 * u + 1]));
        }
        STAMP(ws, 7);
        const unsigned wrd = lds0 + 16384 + ((g >> 1) * 16 + (g & 1) * 4 + (i16 >> 2)) * 160 + 8 * (l & 3) + we * 96;
        f32x4 acc[3][2];
#pragma unroll
        for (int e = 0; e < 3; ++e) { acc[e][0] = f32x4{0.f, 0.f, 0.f, 0.f}; acc[e][1] = f32x4{0.f, 0.f, 0.f, 0.f}; }
        bf16x8 ob[2][2], wf[2][3];
        auto zrd = [&](int n, int i) {
            const unsigned so = (((n >> 2) + 2) & 3) * RSLOT;
            const int k = n & 3;
            if (i < 2) {
                if (n < 16) ob[n & 1][i] = lds_rd16(lds0 + so + (k * 4 + wt * 2 + i) * 1024 + lane16);
                else {
                    const unsigned a = lds0 + so + (k * 8 + 2 * g) * 512 + (wt * 32 + i * 16 + i16) * 8;
                    ob[n & 1][i] = __builtin_shufflevector(*(const LDSP s16x4*)(a), *(const LDSP s16x4*)(a + 512), 0, 1, 2, 3, 4, 5, 6, 7);
                }
            } else {
                const unsigned a = wrd + so + k * 5120 + (i == 4 && we ? 0 : (i - 2) * 32);
                wf[n & 1][i - 2] = lds_tr2(a, a + 1280);
            }
        };
#pragma unroll
        for (int i = 0; i < 5; ++i) zrd(0, i);
#pragma unroll
        for (int n = 0; n < 20; ++n) {
            if (n == 3 || n == 11) LGKM_BAR();
            if (n == Y_DR) { WAIT_VM(0); set_flag(ws, OFF_FY + (((mt * 2 + wt) * 8 + ntile * 2 + we) * 4 + kq) * 64, tag); }
            SB();
#pragma unroll
            for (int e = 0; e < 3; ++e) {
                if (n < 19) { zrd(n + 1, 2 * e); if (e < 2) zrd(n + 1, 2 * e + 1); }
                SB();
#pragma unroll
                for (int t = 0; t < 2; ++t) acc[e][t] = MFMA16(wf[n & 1][e], ob[n & 1][t], acc[e][t]);
                SB();
            }
        }
#pragma unroll
        for (int e = 0; e < 3; ++e) {
            f32x4 z0, z1;
#pragma unroll
            for (int i = 0; i < 4; ++i) {
                const auto r = __builtin_amdgcn_permlane16_swap(__float_as_uint(acc[e][0][i]), __float_as_uint(acc[e][1][i]), false, false);
                z0[i] = __uint_as_float(r[0]); z1[i] = __uint_as_float(r[1]);
            }
            const unsigned za = e < 2 ? zx + e * 2048 : zx2;
            *(LDSP f32x4*)(za) = z0; *(LDSP f32x4*)(za + 1024) = z1;
        }
        STAMP(ws, 10);
        LGKM_BAR();
    }
    STAMP(ws, 9);
}

extern "C" void kernel_launch(void* const* d_in, const int* in_sizes, int n_in, void* d_out, int out_size, void* d_ws,
                              size_t ws_size, hipStream_t stream) {
    (void)hipFuncSetAttribute((const void*)fused_kernel, hipFuncAttributeMaxDynamicSharedMemorySize, LDS_BYTES);
    fused_kernel<<<256, 512, LDS_BYTES, stream>>>((const bf16*)d_in[0], (const bf16*)d_in[1], (const bf16*)d_in[2], (const bf16*)d_in[3],
                                                  (const bf16*)d_in[4], (const bf16*)d_in[6], (const bf16*)d_in[7], (const bf16*)d_in[8],
                                                  (char*)d_ws, (bf16*)d_out);
}
```

```cpp
#include <hip/hip_runtime.h>
#include <hip/hip_bf16.h>
#include <stdint.h>
#define DIAGH 12
#define DIAGW 4

typedef short bf16x8 __attribute__((ext_vector_type(8)));
typedef short s16x4 __attribute__((ext_vector_type(4)));
typedef float f32x16 __attribute__((ext_vector_type(16)));
typedef float f32x4 __attribute__((ext_vector_type(4)));
typedef float f32x2 __attribute__((ext_vector_type(2)));
typedef unsigned u32x4 __attribute__((ext_vector_type(4)));
typedef unsigned u32x2 __attribute__((ext_vector_type(2)));
typedef __bf16 bf16x2_t __attribute__((ext_vector_type(2)));
typedef unsigned long long u64;
typedef __attribute__((address_space(1))) u64 gu64;
typedef __hip_bfloat16 bf16;
#define LDSP __attribute__((address_space(3)))
#define DI __device__ __forceinline__
typedef __amdgpu_buffer_rsrc_t rsrc_t;

constexpr int S_ = 2048, D_ = 640, H_ = 32;
constexpr unsigned OFF_EPOCH = 0;
constexpr unsigned OFF_FQ = 16384, OFF_FAB = 32768, OFF_FO = 49152, OFF_FG = 65536, OFF_FY = 131072;
constexpr unsigned OFF_G = 262144;
constexpr unsigned OFF_Q1 = 524288;
constexpr unsigned OFF_Q2 = OFF_Q1 + H_ * 65536;
constexpr unsigned OFF_K2 = OFF_Q2 + 16 * 32768;
constexpr unsigned KV_CHUNK = 18432, KV_HEAD = 8 * KV_CHUNK;
constexpr unsigned OFF_KV = OFF_K2 + 16 * 32768;
constexpr unsigned OFF_AB = OFF_KV + H_ * KV_HEAD;
constexpr unsigned OFF_Y = OFF_AB + 4 * 64 * 20480;
constexpr unsigned OFF_OM = OFF_Y + 4 * S_ * D_ * 2;
constexpr unsigned OFF_OL = OFF_OM + S_ * 512 * 2;
constexpr unsigned WS_BYTES = OFF_OL + H_ * S_ * 8;
constexpr int LDS_BYTES = 163840;
constexpr int RSLOT = 36864;
constexpr int AT_SLOT = 23744;
constexpr int Q_LDS = 4 * RSLOT, TOUCH_LDS = Q_LDS + 12288;
constexpr unsigned SPIN_CAP = 1u << 16;
constexpr unsigned OOB = 0x7ffffff0u;

#define STAMP(ws, i) do { if ((i) == DIAGH) asm volatile("s_memrealtime %0\n\ts_waitcnt lgkmcnt(0)" : "=s"(t_i) :: "memory"); \
    if ((i) == 9 && blockIdx.x == 37 && wv == DIAGW) while (__builtin_amdgcn_s_memrealtime() - t_e < 10000 + 4 * (t_i - t_e)) __builtin_amdgcn_s_sleep(4); } while (0)
#define MFMA32(a, b, c) __builtin_amdgcn_mfma_f32_32x32x16_bf16((a), (b), (c), 0, 0, 0)
#define MFMA16(a, b, c) __builtin_amdgcn_mfma_f32_16x16x32_bf16((a), (b), (c), 0, 0, 0)
#define WAIT_VM(n) asm volatile("s_waitcnt vmcnt(" #n ")" ::: "memory")
#define TL_LOG() do { } while (0)
#define VM_BAR(n) asm volatile("s_waitcnt vmcnt(" #n ")\n\ts_barrier" ::: "memory")
#define LGKM_BAR() asm volatile("s_waitcnt lgkmcnt(0)\n\ts_barrier" ::: "memory")
#define ALL_BAR() asm volatile("s_waitcnt vmcnt(0) lgkmcnt(0)\n\ts_barrier" ::: "memory")
#define EX2(v) __builtin_amdgcn_exp2f(v)
#define PIN(v) asm volatile("" : "+v"(v))
#define SB() __builtin_amdgcn_sched_barrier(0)
#define RLX_AGENT __ATOMIC_RELAXED, __HIP_MEMORY_SCOPE_AGENT
#define POLL_SLEEP 2
#define L_PRIO 1
#define DFT_RPG 2
#define PRIO_TRICK 1
#define PRIO_U 1
#define AB_RPG 2
#define QKV_ROT 5
#define QKV_DR 3
#define Y_DR 4
#define CODE_PF 36096

DI unsigned pk(float a, float b) { f32x2 v = {a, b}; return __builtin_bit_cast(unsigned, __builtin_convertvector(v, bf16x2_t)); }
DI float bf_lo(unsigned u) { return __uint_as_float(u << 16); }
DI float bf_hi(unsigned u) { return __uint_as_float(u & 0xffff0000u); }
DI rsrc_t mk_rsrc(const void* p, unsigned bytes) { return __builtin_amdgcn_make_buffer_rsrc((void*)p, (short)0, (int)bytes, 0x00020000); }
DI void dma16(unsigned lds_addr, unsigned voff, rsrc_t rs, unsigned soff) {
    asm volatile("s_mov_b32 m0, %0\n\ts_nop 0\n\tbuffer_load_dwordx4 %1, %2, %3 offen lds" ::"s"(lds_addr), "v"(voff), "s"(rs), "s"(soff) : "memory");
}
DI void dma16c(unsigned lds_addr, unsigned voff, rsrc_t rs, unsigned soff) {
    asm volatile("s_mov_b32 m0, %0\n\ts_nop 0\n\tbuffer_load_dwordx4 %1, %2, %3 offen sc1 lds" ::"s"(lds_addr), "v"(voff), "s"(rs), "s"(soff) : "memory");
}
DI void touch(unsigned lds_addr, unsigned voff, rsrc_t rs) {
    asm volatile("s_mov_b32 m0, %0\n\ts_nop 0\n\tbuffer_load_dword %1, %2, 0 offen sc1 lds" ::"s"(lds_addr), "v"(voff), "s"(rs) : "memory");
}
DI bf16x8 lds_rd16(unsigned a) { return *(const LDSP bf16x8*)(a); }
DI s16x4 lds_tr(unsigned a) { return __builtin_amdgcn_ds_read_tr16_b64_v4i16((LDSP s16x4*)(a)); }
DI bf16x8 lds_tr2(unsigned a0, unsigned a1) { return __builtin_shufflevector(lds_tr(a0), lds_tr(a1), 0, 1, 2, 3, 4, 5, 6, 7); }
DI void st16(rsrc_t rs, unsigned off, u32x4 v) { __builtin_amdgcn_raw_buffer_store_b128(v, rs, off, 0, 16); }
DI void st8(rsrc_t rs, unsigned off, u32x2 v) { __builtin_amdgcn_raw_buffer_store_b64(v, rs, off, 0, 16); }
DI void st4(rsrc_t rs, unsigned off, unsigned v) { __builtin_amdgcn_raw_buffer_store_b32(v, rs, off, 0, 16); }
DI u32x4 ld16(rsrc_t rs, unsigned off) { return __builtin_amdgcn_raw_buffer_load_b128(rs, off, 0, 16); }
DI int swap23(int i) { return (i & ~12) | ((i & 4) << 1) | ((i & 8) >> 1); }
DI u32x4 pack8(const f32x4& a, const f32x4& b) { return u32x4{pk(a[0], a[1]), pk(a[2], a[3]), pk(b[0], b[1]), pk(b[2], b[3])}; }
DI u32x4 pack8x(const f32x4& a, const f32x4& b) {
    const auto r0 = __builtin_amdgcn_permlane16_swap(pk(a[0], a[1]), pk(b[0], b[1]), false, false);
    const auto r1 = __builtin_amdgcn_permlane16_swap(pk(a[2], a[3]), pk(b[2], b[3]), false, false);
    return u32x4{r0[0], r1[0], r0[1], r1[1]};
}

DI u64 epoch_tag(char* ws) {
    u64 e;
    asm volatile("s_load_dwordx2 %0, %1, 0x0\n\ts_waitcnt lgkmcnt(0)" : "=s"(e) : "s"(ws + OFF_EPOCH) : "memory");
    return e + 1;
}
DI int lane_here() { int l = threadIdx.x & 63; PIN(l); return l; }
DI void set_flag(char* ws, unsigned off, u64 tag) {
    const int l = lane_here();
    if (l < 8) __hip_atomic_store((gu64*)(ws + off + l * 8), tag, RLX_AGENT);
}
DI void poll2(char* ws, unsigned off0, unsigned off1, u64 tag) {
    _Pragma("unroll 1") for (unsigned spins = 0; spins < SPIN_CAP; ++spins) {
        const u64 a = __hip_atomic_load((gu64*)(ws + off0), RLX_AGENT), b = __hip_atomic_load((gu64*)(ws + off1), RLX_AGENT);
        if (__all(a == tag && b == tag)) break;
        __builtin_amdgcn_s_sleep(POLL_SLEEP);
    }
    asm volatile("" ::: "memory");
}

DI u64 look2(char* ws, unsigned off0, unsigned off1, u64 tag) {
    const u64 a = __hip_atomic_load((gu64*)(ws + off0), RLX_AGENT), b = __hip_atomic_load((gu64*)(ws + off1), RLX_AGENT);
    const u64 m = __ballot(a == tag && b == tag);
    asm volatile("" ::: "memory");
    return m;
}

template <int S0, int NS, int NG, int NR, int RPG, int DR, bool LASTBAR, class RD, class MM>
DI void gemm_loop(RD rd, MM mm) {
    unsigned so = (S0 & 3) * RSLOT;
#pragma unroll
    for (int i = 0; i < NR; ++i) rd(so, 0, i);
    auto half = [&](int kk) {
        SB();
#pragma unroll
        for (int gq = 0; gq < NG; ++gq) {
            mm(kk, gq); SB();
#pragma unroll
            for (int i = gq * RPG; i < (gq + 1) * RPG && i < NR; ++i) rd(so, kk ^ 1, i);
            SB();
        }
        __builtin_amdgcn_s_waitcnt(0xC07F);
    };
    __builtin_amdgcn_s_waitcnt(0xC07F);
    _Pragma("unroll 1") for (int it = 0; it < NS - 1; ++it) {
        half(0);
        if (it == DR - 1) WAIT_VM(0);
        asm volatile("s_barrier" ::: "memory");
        so = so == 3 * RSLOT ? 0 : so + RSLOT;
        half(1);
    }
    half(0);
    if (LASTBAR) asm volatile("s_barrier" ::: "memory");
#pragma unroll
    for (int gq = 0; gq < NG; ++gq) mm(1, gq);
}

DI void ab_compute(bool isb, int m0, int n0, rsrc_t rws, unsigned lds0, int wv) {
    const int l = threadIdx.x & 63, i16 = l & 15, g = l >> 4;
    const unsigned xrd = lds0 + (wv * 32 + i16) * 128 + ((g ^ (i16 & 7)) << 4), wrd = lds0 + 16384 + i16 * 128 + ((g ^ (i16 & 7)) << 4);
    f32x4 acc[2][5];
#pragma unroll
    for (int j = 0; j < 5; ++j) { acc[0][j] = f32x4{0.f, 0.f, 0.f, 0.f}; acc[1][j] = f32x4{0.f, 0.f, 0.f, 0.f}; }
    bf16x8 xf[2][2], wf[2][5];
    unsigned xa, wa;
    gemm_loop<10, 10, 5, 7, AB_RPG, QKV_DR, false>(
        [&](unsigned so, int kk, int i) {
            if (i == 0) { xa = (xrd ^ (kk << 6)) + so; PIN(xa); }
            if (i == 2) { wa = (wrd ^ (kk << 6)) + so; PIN(wa); }
            if (i < 2) xf[kk][i] = lds_rd16(xa + i * 2048);
            else wf[kk][i - 2] = lds_rd16(wa + (i - 2) * 2048);
        },
        [&](int kk, int j) {
            acc[0][j] = MFMA16(xf[kk][0], wf[kk][j], acc[0][j]);
            acc[1][j] = MFMA16(xf[kk][1], wf[kk][j], acc[1][j]);
        });
    const float sg = isb ? -1.f : 1.f;
    const int ntile = n0 / 160, e16 = (n0 - ntile * 160) >> 4, k32 = ((isb ? 2048 : 0) + m0 + 32 * wv) >> 5;
#pragma unroll
    for (int j = 0; j < 5; ++j)
        st16(rws, OFF_AB + (unsigned)(((ntile * 128 + k32) * 10 + e16 + j) * 1024 + l * 16), pack8(acc[0][j] * sg, acc[1][j] * sg));
}

template <int KIND>
DI void qkv_compute(int m0, int nt, rsrc_t rws, unsigned lds0, int wv) {
    const int l = threadIdx.x & 63, wm = wv & 1, wn = wv >> 1, r32 = l & 31, hh = l >> 5, q = (l >> 2) & 3, p = l & 3;
    const unsigned xrd = lds0 + (wm * 64 + r32) * 128 + ((hh ^ ((r32 >> 1) & 7)) << 4);
    const int pq = KIND == 1 ? ((p & 1) << 1) | (p >> 1) : p;
    const unsigned wrd = lds0 + 16384 + (8 * hh + q) * 256 + (((2 * wn) ^ q) << 6) + ((l >> 4) & 1) * 32 + 8 * pq;
    const bool isq = nt < 5;
    unsigned off[2][2][2];
#pragma unroll
    for (int j = 0; j < 2; ++j)
#pragma unroll
        for (int s2 = 0; s2 < 2; ++s2) {
            if (KIND == 2) {
                const int col = (nt - 10) * 128 + wn * 64 + 32 * j + 16 * s2 + (l & 15), s = m0 + wm * 64;
                const int h = col / 20, d = col - h * 20;
                const int vp = d < 8 ? d : (d < 18 ? d - 8 : d - 10), vh = (d >= 4 && d < 12) || d == 16 || d == 17;
                off[j][s2][0] = nt >= 15 ? OOB : OFF_KV + (unsigned)(h * KV_HEAD + 8192 + vp * 1024 + vh * 512 + ((l >> 4) & 1) * 32 + hh * 16 + (s >> 8) * KV_CHUNK + (s & 255) * 2);
                off[j][s2][1] = off[j][s2][0] + 64;
            } else {
                const int pos = isq ? swap23(r32) : r32;
                const int c = (isq ? nt : nt - 5) * 128 + wn * 64 + 32 * j + 16 * s2 + 8 * hh;
                const int pair = c / 40, oc = (c - pair * 40) >> 3;
                const int h = 2 * pair + (oc > 2), half = oc > 2 ? oc - 3 : oc, tile = (m0 + wm * 64) >> 5;
                const unsigned o2 = (isq ? OFF_Q2 : OFF_K2) + pair * 32768 + (tile * 32 + pos) * 16;
                const unsigned o1 = (isq ? OFF_Q1 + h * 65536 + tile * 1024 : OFF_KV + h * KV_HEAD + (tile >> 3) * KV_CHUNK + (tile & 7) * 1024) + (half * 32 + pos) * 16;
                off[j][s2][0] = oc == 2 ? o2 : o1;
                off[j][s2][1] = oc == 2 ? o2 + 512 : o1 + 1024;
            }
            PIN(off[j][s2][0]); PIN(off[j][s2][1]);
        }
    f32x16 acc[2][2];
#pragma unroll
    for (int i = 0; i < 16; ++i) { acc[0][0][i] = 0.f; acc[0][1][i] = 0.f; acc[1][0][i] = 0.f; acc[1][1][i] = 0.f; }
    bf16x8 xf[2][2][2], wf[2][2][2];
    unsigned xa[2], wa[2];
    asm volatile("s_barrier" ::: "memory");
    gemm_loop<0, 10, 8, 8, 2, 0, true>(
        [&](unsigned so, int kk, int i) {
            const int k2 = i >> 2, j = i & 1;
            if (i == 0) { xa[0] = (xrd ^ (kk << 6)) + so; xa[1] = xa[0] ^ 32; wa[0] = wrd + so; wa[1] = wa[0] ^ 64; PIN(xa[0]); PIN(xa[1]); PIN(wa[0]); PIN(wa[1]); }
            if (i & 2) { const unsigned a = wa[j] + (kk * 2 + k2) * 4096; wf[kk][k2][j] = lds_tr2(a, a + 1024); }
            else xf[kk][k2][j] = lds_rd16(xa[k2] + j * 4096);
        },
        [&](int kk, int gq) {
            const int k2 = gq >> 2, m = (gq >> 1) & 1, j = gq & 1;
            acc[m][j] = (KIND == 1) ? MFMA32(wf[kk][k2][j], xf[kk][k2][m], acc[m][j]) : MFMA32(xf[kk][k2][m], wf[kk][k2][j], acc[m][j]);
        });
    const float sc = isq ? 0.22360679774997896f * 1.4426950408889634f : 1.f;
#pragma unroll
    for (int m = 0; m < 2; ++m)
#pragma unroll
        for (int j = 0; j < 2; ++j) {
            const f32x16& a = acc[m][j];
            u32x4 lo, hi;
#pragma unroll
            for (int i = 0; i < 4; ++i) {
                if (KIND == 2) {
                    const auto r = __builtin_amdgcn_permlane16_swap(pk(a[2 * i], a[2 * i + 1]), pk(a[8 + 2 * i], a[9 + 2 * i]), false, false);
                    lo[i] = r[0]; hi[i] = r[1];
                } else {
                    lo[i] = pk(a[2 * i] * sc, a[2 * i + 1] * sc); hi[i] = pk(a[8 + 2 * i] * sc, a[9 + 2 * i] * sc);
                }
            }
            st16(rws, off[j][0][m], lo);
            st16(rws, off[j][1][m], hi);
        }
}

DI float half_swap_max(float m) {
    auto r = __builtin_amdgcn_permlane32_swap(__float_as_uint(m), __float_as_uint(m), false, false);
    return fmaxf(__uint_as_float(r[0]), __uint_as_float(r[1]));
}
DI float max16(const f32x16& s) {
    float a = fmaxf(fmaxf(s[0], s[1]), s[2]), b = fmaxf(fmaxf(s[3], s[4]), s[5]);
    a = fmaxf(fmaxf(a, s[6]), s[7]); b = fmaxf(fmaxf(b, s[8]), s[9]);
    a = fmaxf(fmaxf(a, s[10]), s[11]); b = fmaxf(fmaxf(b, s[12]), s[13]);
    a = fmaxf(fmaxf(a, s[14]), s[15]);
    return fmaxf(a, b);
}

__global__ __launch_bounds__(512, 2) void fused_kernel(const bf16* x, const bf16* cr, const bf16* ci, const bf16* wr, const bf16* wi,
                                                       const bf16* wqkv, const bf16* wo, const bf16* gw, char* ws, bf16* out) {
    u64 t_e; asm volatile("s_memrealtime %0\n\ts_waitcnt lgkmcnt(0)" : "=s"(t_e)); u64 t_i = t_e;
    extern __shared__ __attribute__((aligned(16))) char smem[];
    const unsigned lds0 = (unsigned)(uintptr_t)(LDSP char*)smem;
    const int b = blockIdx.x, xcd = b & 7, slot = b >> 3, l = threadIdx.x & 63;
    const int wv = __builtin_amdgcn_readfirstlane(threadIdx.x >> 6);
    const int mt1 = (xcd & 1) * 8 + (slot & 7), nt1 = (xcd >> 1) * 4 + (slot >> 3);
    const int head = xcd * 4 + (slot >> 3), qb = slot & 7, pair = head >> 1, odd = head & 1;
    const int kq = xcd & 3, mt = (xcd >> 2) * 8 + (slot >> 2), ntile = slot & 3;
    const int mt3 = xcd * 4 + (slot >> 3), nt3 = slot & 7;
    const rsrc_t rws = mk_rsrc(ws, WS_BYTES);
    const unsigned lane16 = l * 16;
    const int hh = l >> 5, r32 = l & 31, i16 = l & 15, g = l >> 4;
    const int qg = wv & 3, kh = wv >> 2;
    const unsigned swz = (unsigned)(((l & 7) ^ (l >> 3)) << 4);
    u64 tag;
    bool kv_all = true;
    const unsigned kvsrc = OFF_KV + head * KV_HEAD + qg * 1024, k2src = OFF_K2 + pair * 32768 + qg * 1024;
    auto kv_piece = [&](int i, int c, int sl) {
        const unsigned dst = lds0 + sl * AT_SLOT, src = kvsrc + c * KV_CHUNK;
        if (i < 2) dma16c(dst + qg * 1024 + i * 4096, lane16, rws, src + i * 4096);
        else if (i == 2) dma16c(dst + 8192 + qg * 1024, lane16, rws, k2src + c * 4096);
        else if (i < 5 || qg < 2) dma16c(dst + 12288 + (qg + (i - 3) * 4) * 1040, lane16, rws, src + 8192 + (i - 3) * 4096);
    };
    unsigned voff, koff, k2off, coff, tch_c, tch_w, tch_a, kvv2;
    auto setup = [&]() {
        const bool vdata = r32 < 18 || r32 == 20 || r32 == 21, vone = r32 == 18 || r32 == 22;
        const int vp = r32 < 4 ? r32 : (r32 < 12 ? r32 - 4 : (r32 < 18 ? r32 - 8 : r32 - 12)), vh = (r32 >= 4 && r32 < 12) || r32 == 16 || r32 == 17;
        voff = 12288 + hh * 16 + kh * 256 + (vdata ? vp * 1040 + vh * 512 : (vone ? 10400 : 10928));
        koff = lane16 + kh * 4096; k2off = 8192 + lane16 + kh * 2048;
        coff = (unsigned)((mt * 128 + 32 * qg + (l >> 3)) * 4096 + (kq & 1) * 2048) + swz;
        tch_c = l < 16 ? (unsigned)((mt * 128 + ntile * 32 + wv * 4) * 4096 + (kq & 1) * 2048 + l * 128) : OOB;
        tch_w = l < 25 ? (unsigned)(((slot * 8 + wv) * 25 + l) * 128) : OOB;
        tch_a = l < 40 ? OFF_AB + (unsigned)((ntile * 64 + kq * 16) * 20480 + (((mt & 7) * 8 + wv) * 40 + l) * 128) : OOB;
        kvv2 = wv < 6 ? lane16 : OOB;
        PIN(voff); PIN(koff); PIN(k2off); PIN(coff); PIN(tch_c); PIN(tch_w); PIN(tch_a); PIN(kvv2);
    };
    int tl_n = 1;
    const char* pc;
    asm volatile("s_getpc_b64 %0" : "=s"(pc));

    if (__builtin_expect(kh, 1)) {
        __builtin_amdgcn_s_setprio(L_PRIO);
        const rsrc_t rx = mk_rsrc(x, S_ * D_ * 2), rw = mk_rsrc(wqkv, D_ * 1920 * 2);
        const unsigned xoff = (unsigned)((mt1 * 128 + 32 * qg + 8 * hh + ((l >> 3) & 3)) * (D_ * 2)) + swz;
        const int k0 = QKV_ROT == 5 ? (2 * (slot & 7) + (((slot >> 3) * 5 + 1) >> 1)) % 10 : QKV_ROT == 2 ? 5 * ((slot ^ (slot >> 3)) & 1) : 0;
        const unsigned xoffq = (unsigned)((mt1 * 128 + 64 * (qg >> 1) + 4 * (qg & 1) + 8 * hh + ((l >> 3) & 3)) * (D_ * 2) + (((l & 7) ^ (4 * (qg & 1) + g)) << 4));
        const unsigned woff = (unsigned)((16 * qg + g) * 3840 + (((i16 >> 2) ^ g) << 6) + ((i16 & 3) << 4) + (nt1 < 15 ? nt1 : 14) * 256);
        auto x_part = [&](int ks, unsigned dst) {
#pragma unroll
            for (int i = 0; i < 4; ++i) dma16(dst + (qg * 4 + i) * 1024, xoff, rx, ks * 128 + (16 * (i & 1) + 4 * (i >> 1)) * (D_ * 2));
        };
        auto issue_qkv = [&](int k) {
            const int ks = k0 + k - (k0 + k >= 10 ? 10 : 0);
            const unsigned dst = lds0 + (k & 3) * RSLOT;
#pragma unroll
            for (int i = 0; i < 4; ++i) dma16(dst + (8 * (qg >> 1) + (qg & 1) + 2 * i) * 1024, xoffq, rx, ks * 128 + 16 * i * (D_ * 2));
#pragma unroll
            for (int i = 0; i < 4; ++i) dma16(dst + 16384 + (qg * 4 + i) * 1024, woff, rw, (ks * 64 + 4 * i) * 3840);
        };
        if (qg == 0) touch(lds0 + TOUCH_LDS, l < 9 ? (unsigned)((l * 32 + slot) * 128) : OOB, mk_rsrc(pc, CODE_PF));
        issue_qkv(0); issue_qkv(1);
        tag = epoch_tag(ws);
        VM_BAR(8); STAMP(ws, 12); issue_qkv(2); issue_qkv(3);
        const rsrc_t rd = mk_rsrc(nt1 >= 8 ? wi : wr, D_ * D_ * 2);
        const unsigned doff = (unsigned)(((nt1 & 7) * 80 + 24 * qg + (l >> 3)) * (D_ * 2)) + swz;
        const int n0 = (pair * 40) >> 7, n1 = (pair * 40 + 39) >> 7, sec = (l >> 2) & 3, sb5 = sec == 3 ? 0 : sec * 5;
        const int frow = hh ? (5 + (g & 1) * 5) * 16 + i16 : sb5 * 16 + ((qb * 2 + (sec == 1 || sec == 2 ? (l & 3) : (l & 1))) & 15);
        const unsigned fq0 = OFF_FQ + (n0 * 16 + frow) * 64, fq1 = OFF_FQ + (n1 * 16 + frow) * 64, look = lds0 + 2 * RSLOT + 34816;
        unsigned dv1 = qg == 3 ? OOB : doff, dv2 = dv1;
        const rsrc_t rd2 = mk_rsrc(qg == 3 ? (const void*)ws : (const void*)(nt1 >= 8 ? wi : wr), qg == 3 ? +WS_BYTES : D_ * D_ * 2u);
        auto issue_ab = [&](int k) {
            const unsigned dst = lds0 + ((k + 2) & 3) * RSLOT;
            x_part(k, dst);
            dma16(dst + 16384 + qg * 3072, doff, rd, k * 128);
            dma16c(qg == 3 ? look : dst + 16384 + qg * 3072 + 1024, dv1, rd2, qg == 3 ? 0 : k * 128 + 8 * (D_ * 2));
            dma16c(qg == 3 ? look + 1024 : dst + 16384 + qg * 3072 + 2048, dv2, rd2, qg == 3 ? 0 : k * 128 + 16 * (D_ * 2));
        };
        auto seen_lds = [&]() { return tag ? __ballot(*(volatile LDSP u64*)(look + lane16) == tag && *(volatile LDSP u64*)(look + 1024 + lane16) == tag) : 0ull; };
        touch(lds0 + TOUCH_LDS, l < 25 ? (unsigned)((((xcd >> 1) & 1) * 3200 + (slot * 4 + qg) * 25 + l) * 128) : OOB, rd);
        VM_BAR(17); issue_qkv(4);
        VM_BAR(17); issue_qkv(5);
        _Pragma("unroll 1") for (int s = 3; s < 7; ++s) { VM_BAR(16); issue_qkv(s + 3); }
        VM_BAR(16); issue_ab(0);
        VM_BAR(15); issue_ab(1);
        VM_BAR(14); issue_ab(2);
        VM_BAR(14); issue_ab(3);
        {
            const int lg = lane_here();
            const rsrc_t rg = mk_rsrc(gw, D_ * 2);
#pragma unroll
            for (int i = 0; i < 3; ++i) dma16(lds0 + (2 + (qg >> 1)) * RSLOT + 28672 + (qg & 1) * 3072 + i * 1024, i < 2 || lg < 32 ? lane16 : OOB, rx, (b * 8 + qg * 2) * (D_ * 2) + i * 1024);
            dma16(lds0 + TOUCH_LDS + 1024, lane16, rg, 0);
            dma16(lds0 + TOUCH_LDS + 2048, lane16 + 1024, rg, 0);
        }
        setup();
        _Pragma("unroll 1") for (int s = 11; s < 17; ++s) {
            VM_BAR(14);
            if (s == 10 + QKV_DR && qg == 0) set_flag(ws, OFF_FQ + (nt1 * 16 + mt1) * 64, tag);
            if (s == 16 && qg == 3) { dv1 = fq0; dv2 = fq1; }
            issue_ab(s - 7);
        }
        VM_BAR(14);
        VM_BAR(7);
        u64 seen = seen_lds();
        const bool early = (unsigned)seen == 0xffffffffu;
        if (!early) {
            VM_BAR(0); seen = seen_lds();
            _Pragma("unroll 1") for (unsigned spins = 0; spins < SPIN_CAP && (unsigned)seen != 0xffffffffu; ++spins) {
                seen = look2(ws, fq0, fq1, tag);
                __builtin_amdgcn_s_sleep(POLL_SLEEP);
            }
        }
        STAMP(ws, 2);
        dma16c(lds0 + Q_LDS + qg * 2048, lane16, rws, OFF_Q1 + head * 65536 + (qb * 8 + qg * 2) * 1024);
        dma16c(lds0 + Q_LDS + qg * 2048 + 1024, lane16, rws, OFF_Q1 + head * 65536 + (qb * 8 + qg * 2 + 1) * 1024);
        dma16c(lds0 + Q_LDS + 8192 + qg * 1024, lane16, rws, OFF_Q2 + pair * 32768 + (qb * 4 + qg) * 1024);
#pragma unroll
        for (int i = 0; i < 6; ++i) kv_piece(i, qb, 0);
        if (early) VM_BAR(8);
#pragma unroll
        for (int i = 0; i < 6; ++i) kv_piece(i, (qb + 1) & 7, 1);
        kv_all = seen == ~0ull;
        if (qg < 3) {
            const unsigned c = hh ? 0u : 0x3F803F80u;
            *(LDSP u32x4*)(lds0 + qg * AT_SLOT + 12288 + 10400 + lane16 + hh * 16) = u32x4{c, c, c, c};
        }
        float gs = 0.f;
#pragma unroll
        for (int i = 0; i < 3; ++i) {
            const unsigned c = i < 2 || r32 < 16 ? (r32 + 32 * i) * 16 : 0;
            const u32x4 gx = __builtin_bit_cast(u32x4, lds_rd16(lds0 + (2 + (qg >> 1)) * RSLOT + 28672 + (qg & 1) * 3072 + hh * 1280 + c));
            const u32x4 gg = __builtin_bit_cast(u32x4, lds_rd16(lds0 + TOUCH_LDS + 1024 + c));
            float t = 0.f;
#pragma unroll
            for (int j = 0; j < 4; ++j) t += bf_lo(gx[j]) * bf_lo(gg[j]) + bf_hi(gx[j]) * bf_hi(gg[j]);
            gs += i < 2 || r32 < 16 ? t : 0.f;
        }
#pragma unroll
        for (int off = 16; off > 0; off >>= 1) gs += __shfl_xor(gs, off);
        if (r32 < 16) st4(rws, OFF_G + (b * 8 + qg * 2 + hh) * 64 + r32 * 4, __float_as_uint(1.f / (1.f + __expf(-gs))));
        __builtin_amdgcn_s_setprio(0);
        asm volatile("s_waitcnt vmcnt(6) lgkmcnt(0)\n\ts_barrier" ::: "memory");
    } else {
        tag = epoch_tag(ws);
        setup();
        STAMP(ws, 1);
        if (nt1 < 10) qkv_compute<1>(mt1 * 128, nt1, rws, lds0, wv);
        else qkv_compute<2>(mt1 * 128, nt1, rws, lds0, wv);
        STAMP(ws, 2);
        ab_compute(nt1 >= 8, mt1 * 128, (nt1 & 7) * 80, rws, lds0, wv);
        STAMP(ws, 3);
        asm volatile("s_barrier" ::: "memory");
    }
    TL_LOG();
    STAMP(ws, 4);

    const bf16* cmat = kq < 2 ? cr : ci;
    const rsrc_t rc = mk_rsrc(cmat, S_ * S_ * 2), rwo = mk_rsrc(wo, D_ * D_ * 2);
    const unsigned absrc = OFF_AB + (unsigned)((ntile * 64 + kq * 16) * 20480 + qg * 5120);
    auto dft_piece = [&](int i, int k) {
        const unsigned dst = lds0 + ((k + 2) & 3) * RSLOT;
        if (i < 5) dma16c(dst + 16384 + (qg * 5 + i) * 1024, lane16, rws, absrc + k * 20480 + i * 1024);
        else dma16(dst + (qg * 4 + i - 5) * 1024, coff, rc, k * 128 + (i - 5) * 8 * 4096);
    };
    bf16x8 q1[2], q2[2][2];
#pragma unroll
    for (int X = 0; X < 2; ++X) {
        q1[X] = lds_rd16(lds0 + Q_LDS + (qg * 2 + X) * 1024 + lane16);
        const u32x4 qr = __builtin_bit_cast(u32x4, lds_rd16(lds0 + Q_LDS + 8192 + ((qg * 2 + X) * 32 + r32) * 16));
#pragma unroll
        for (int P = 0; P < 2; ++P) {
            const bool lo = hh == P && !odd, hi = hh == P && odd;
            q2[X][P] = __builtin_bit_cast(bf16x8, u32x4{lo ? qr[0] : 0u, lo ? qr[1] : 0u, hi ? qr[2] : 0u, hi ? qr[3] : 0u});
        }
    }
    const f32x16 zero16 = {0.f, 0.f, 0.f, 0.f, 0.f, 0.f, 0.f, 0.f, 0.f, 0.f, 0.f, 0.f, 0.f, 0.f, 0.f, 0.f};
    unsigned sb = lds0;
    bf16x8 k1f[2], k2f, v0f, v1f;
    k1f[0] = lds_rd16(sb + koff); k2f = lds_rd16(sb + k2off); k1f[1] = lds_rd16(sb + koff + 1024);
    v0f = lds_rd16(sb + voff); v1f = v0f;
    f32x16 sc, negm[2], o[2];
    float m0[2];
    {
        f32x16 sB = MFMA32(k1f[0], q1[1], zero16);
        sB = MFMA32(k2f, q2[1][0], sB);
        sc = MFMA32(k1f[0], q1[0], zero16);
        sc = MFMA32(k2f, q2[0][0], sc);
        m0[1] = half_swap_max(max16(sB));
        m0[0] = half_swap_max(max16(sc));
#pragma unroll
        for (int i = 0; i < 16; ++i) { negm[0][i] = -m0[0]; negm[1][i] = -m0[1]; sc[i] -= m0[0]; }
    }
    o[0] = zero16; o[1] = zero16;
    u32x4 p0, p1 = {0u, 0u, 0u, 0u};
    float e12 = 0.f, e13 = 0.f, e14 = 0.f, e15 = 0.f;
    const unsigned kvs0 = OFF_KV + head * KV_HEAD + wv * 1024;
    const unsigned kvs2 = wv < 2 ? kvs0 + 16384 : OFF_K2 + pair * 32768 + (wv - 2) * 1024, kvstep2 = wv < 2 ? KV_CHUNK : 4096;
    const unsigned kvd2 = wv < 2 ? 12288 + (8 + wv) * 1040 : 8192 + ((wv - 2) & 3) * 1024;
    const int rare = wv == 4 ? 0x32 | (kv_all ? 0 : 1) | (b == 0 ? 8 : 0) : 0;
    int sl = 0;
    if (PRIO_TRICK) __builtin_amdgcn_s_setprio(1);
    STAMP(ws, 11);
#pragma unroll
    for (int last = 0; last < 2; ++last)
    _Pragma("unroll 1") for (int c = last ? 6 : 0; c < (last ? 8 : 6); ++c) {
        const int nsl = sl == 2 ? 0 : sl + 1;
        const unsigned sbn = lds0 + nsl * AT_SLOT;
#pragma unroll
        for (int u = 0; u < 8; ++u) {
            const int T = u >> 1, X = u & 1, Tn = (u + 1) >> 1, Xn = X ^ 1;
            if (u == 4) {
                if (!last && ((rare >> c) & 1)) {
                    const int lh = lane_here();
                    if (c == 0) {
                        const int s5 = 5 + ((lh >> 4) & 1) * 5;
                        poll2(ws, OFF_FQ + ((s5 + ((pair * 40) >> 7)) * 16 + (lh & 15)) * 64, OFF_FQ + ((s5 + ((pair * 40 + 39) >> 7)) * 16 + (lh & 15)) * 64, tag);
                    }
                    if (c == 3) {
                        poll2(ws, OFF_FQ + lh * 64, OFF_FQ + (64 + lh) * 64, tag);
                        poll2(ws, OFF_FQ + (128 + lh) * 64, OFF_FQ + (192 + lh) * 64, tag);
                        set_flag(ws, OFF_EPOCH, tag);
                    }
                    const unsigned fa = lh < 32 ? OFF_FAB + (((kq >> 1) * 8 + ntile * 2 + ((lh >> 3) & 1)) * 16 + (kq & 1) * 8 + (lh & 7)) * 64 : OFF_FG + (mt3 * 8 + (lh & 7)) * 64;
                    WAIT_VM(0);
                    if (c == 1) { set_flag(ws, OFF_FAB + (nt1 * 16 + mt1) * 64, tag); set_flag(ws, OFF_FG + b * 64, tag); }
                    if (c == 4) dma16c(lds0 + TOUCH_LDS + 1024, fa, rws, 0);
                    if (c == 5 && !__all(*(volatile LDSP u64*)(lds0 + TOUCH_LDS + 1024 + lh * 16) == tag)) poll2(ws, fa, fa, tag);
                } else WAIT_VM(0);
                VM_BAR(63);
                if (PRIO_TRICK) __builtin_amdgcn_s_setprio(1);
            }
            if (PRIO_TRICK && (u == PRIO_U || (last && u == 4 + PRIO_U && c == 7))) __builtin_amdgcn_s_setprio(0);
            float a0, a1, a2, a3, b0, b1, b2, b3;
            SB();
            f32x16 sn = MFMA32(k1f[Tn & 1], q1[Xn], negm[Xn]);
            if (X) k1f[(Tn + 1) & 1] = Tn + 1 < 4 ? lds_rd16(sb + koff + (Tn + 1) * 1024) : lds_rd16(sbn + koff + (Tn - 3) * 1024);
            p1[2] = pk(e12, e13); p1[3] = pk(e14, e15); PIN(p1);
            a0 = EX2(sc[0]); a1 = EX2(sc[1]); a2 = EX2(sc[2]); a3 = EX2(sc[3]); PIN(a0); PIN(a1); PIN(a2); PIN(a3);
            SB();
            sn = MFMA32(k2f, q2[Xn][Tn & 1], sn);
            if (u == 2) k2f = lds_rd16(sb + k2off + 1024);
            if (u == 6) k2f = lds_rd16(sbn + k2off);
            p0[0] = pk(a0, a1); p0[1] = pk(a2, a3);
            b0 = EX2(sc[4]); b1 = EX2(sc[5]); b2 = EX2(sc[6]); b3 = EX2(sc[7]); PIN(b0); PIN(b1); PIN(b2); PIN(b3);
            SB();
            if (last && u == 4 && !kh) dft_piece(4, c - 6);
            o[Xn] = MFMA32(v1f, __builtin_bit_cast(bf16x8, p1), o[Xn]);
            if (!X) v1f = lds_rd16(sb + voff + T * 64 + 32);
            p0[2] = pk(b0, b1); p0[3] = pk(b2, b3); PIN(p0);
            a0 = EX2(sc[8]); a1 = EX2(sc[9]); a2 = EX2(sc[10]); a3 = EX2(sc[11]); PIN(a0); PIN(a1); PIN(a2); PIN(a3);
            SB();
            o[X] = MFMA32(v0f, __builtin_bit_cast(bf16x8, p0), o[X]);
            if (X) v0f = T < 3 ? lds_rd16(sb + voff + (T + 1) * 64) : lds_rd16(sbn + voff);
            e12 = EX2(sc[12]); e13 = EX2(sc[13]); e14 = EX2(sc[14]); e15 = EX2(sc[15]); PIN(e12); PIN(e13); PIN(e14); PIN(e15);
            SB();
            p1[0] = pk(a0, a1); p1[1] = pk(a2, a3);
            SB();
            if (!last) {
                if (u >= 4) {
                    int cu = c, su = sl;
                    asm volatile("" : "+s"(cu), "+s"(su));
                    const int cn = (qb + cu + 2) & 7;
                    const unsigned sbl = lds0 + (su == 0 ? 2 : su - 1) * AT_SLOT;
                    if (u == 4) dma16c(sbl + wv * 1024, lane16, rws, kvs0 + cn * KV_CHUNK);
                    if (u == 5) dma16c(sbl + 12288 + wv * 1040, lane16, rws, kvs0 + cn * KV_CHUNK + 8192);
                    if (u == 6) dma16c(wv < 6 ? sbl + kvd2 : lds0 + TOUCH_LDS, kvv2, rws, kvs2 + cn * kvstep2);
                    if (u == 7) touch(lds0 + TOUCH_LDS, cu < 4 ? tch_c + cu * 4096 : (cu == 4 ? +tch_w : +tch_a),
                                      mk_rsrc(cu < 4 ? (const void*)cmat : (cu == 4 ? (const void*)wo : (const void*)ws),
                                              cu < 4 ? S_ * S_ * 2u : (cu == 4 ? D_ * D_ * 2u : +WS_BYTES)));
                }
            } else if (u >= 4) {
                const unsigned dst = lds0 + (c - 4) * RSLOT;
                const int i = kh * 2 + (u & 1);
                if (u < 6) dma16c(dst + 16384 + (qg * 5 + i) * 1024, lane16, rws, absrc + (c - 6) * 20480 + i * 1024);
                else dma16(dst + (qg * 4 + i) * 1024, coff, rc, (c - 6) * 128 + i * 8 * 4096);
            }
            sc = sn;
        }
        sl = nsl; sb = sbn;
    }
    p1[2] = pk(e12, e13); p1[3] = pk(e14, e15);
    o[1] = MFMA32(v1f, __builtin_bit_cast(bf16x8, p1), o[1]);
    STAMP(ws, 5);
    {
        f32x16 of;
        const f32x16 ox = kh ? o[0] : o[1], om = kh ? o[1] : o[0];
        const float mx = kh ? m0[0] : m0[1], mm = kh ? m0[1] : m0[0];
        const unsigned xw = lds0 + 2 * AT_SLOT + wv * 3072 + lane16, xr = lds0 + 2 * AT_SLOT + (wv ^ 4) * 3072 + lane16;
        *(LDSP f32x4*)(xw) = f32x4{ox[0], ox[1], ox[2], ox[3]};
        *(LDSP f32x4*)(xw + 1024) = f32x4{ox[4], ox[5], ox[6], ox[7]};
        *(LDSP f32x4*)(xw + 2048) = f32x4{ox[8], ox[9], ox[10], mx};
        ALL_BAR(); TL_LOG();
        if (kh) {
#pragma unroll
            for (int i = 0; i < 9; ++i) dft_piece(i, 2);
        }
        const f32x4 r0 = *(const LDSP f32x4*)(xr), r1 = *(const LDSP f32x4*)(xr + 1024), r2 = *(const LDSP f32x4*)(xr + 2048);
        const float mn = fmaxf(mm, r2[3]), fa = EX2(mm - mn), fb = EX2(r2[3] - mn);
#pragma unroll
        for (int i = 0; i < 4; ++i) { of[i] = om[i] * fa + r0[i] * fb; of[4 + i] = om[4 + i] * fa + r1[i] * fb; }
#pragma unroll
        for (int i = 0; i < 3; ++i) of[8 + i] = om[8 + i] * fa + r2[i] * fb;
        float asum = 0.f;
#pragma unroll
        for (int i = 0; i < 10; ++i) asum += fabsf(of[i]);
        const bool bad = !(om[10] <= 1.0e18f) || !(r2[2] <= 1.0e18f) || !(asum <= 1.0e37f);
        if (__builtin_expect(__any(bad), 0)) {
            const bf16x8 fq1 = kh ? q1[1] : q1[0], fq2 = kh ? q2[1][0] : q2[0][0];
            const int rr = lane_here() & 31;
            const bool vdata = rr < 18 || rr == 20 || rr == 21, vone = rr == 18 || rr == 22;
            const int vp = rr < 4 ? rr : (rr < 12 ? rr - 4 : (rr < 18 ? rr - 8 : rr - 12)), vh = (rr >= 4 && rr < 12) || rr == 16 || rr == 17;
            const unsigned vg = 8192 + hh * 16 + (vdata ? vp * 1024 + vh * 512 : 0), vc = vone ? 0x3F803F80u : 0u;
            const u32x4 vconst = {vc, vc, vc, vc};
            float m = -INFINITY;
            of = zero16;
            _Pragma("unroll 1") for (int t = 0; t < 64; ++t) {
                const unsigned cb = OFF_KV + head * KV_HEAD + (t >> 3) * KV_CHUNK;
                const int T = t & 7;
                f32x16 s = MFMA32(__builtin_bit_cast(bf16x8, ld16(rws, cb + T * 1024 + lane16)), fq1, zero16);
                s = MFMA32(__builtin_bit_cast(bf16x8, ld16(rws, OFF_K2 + pair * 32768 + (t * 32 + r32) * 16)), fq2, s);
                const float mq = fmaxf(m, half_swap_max(max16(s)));
                const float al = EX2(m - mq);
                m = mq;
#pragma unroll
                for (int i = 0; i < 16; ++i) { of[i] *= al; s[i] = EX2(s[i] - mq); }
                const u32x4 pa = {pk(s[0], s[1]), pk(s[2], s[3]), pk(s[4], s[5]), pk(s[6], s[7])};
                const u32x4 pb = {pk(s[8], s[9]), pk(s[10], s[11]), pk(s[12], s[13]), pk(s[14], s[15])};
                const u32x4 va = ld16(rws, cb + vg + T * 64), vb2 = ld16(rws, cb + vg + T * 64 + 32);
                of = MFMA32(__builtin_bit_cast(bf16x8, vdata ? va : vconst), __builtin_bit_cast(bf16x8, pa), of);
                of = MFMA32(__builtin_bit_cast(bf16x8, vdata ? vb2 : vconst), __builtin_bit_cast(bf16x8, pb), of);
            }
        }
        const float rl = 1.f / of[10];
        const int q = (qb * 8 + qg * 2 + kh) * 32 + r32;
        st16(rws, OFF_OM + ((q >> 4) * 16 + pair) * 1024 + (odd * 2 + hh) * 256 + (q & 15) * 16,
             u32x4{pk(of[0] * rl, of[1] * rl), pk(of[2] * rl, of[3] * rl), pk(of[4] * rl, of[5] * rl), pk(of[6] * rl, of[7] * rl)});
        const unsigned xl = pk(of[8] * rl, of[9] * rl);
        const auto rx = __builtin_amdgcn_permlane32_swap(xl, xl, false, false);
        if (!hh) st8(rws, OFF_OL + (head * S_ + q) * 8, u32x2{rx[0], rx[1]});
    }
    STAMP(ws, 6);

    const int wt = qg & 1, we = qg >> 1;
    const unsigned zx = lds0 + Q_LDS + qg * 4096 + lane16;
    const unsigned zx2 = lds0 + 3 * RSLOT + qg * 2048 + lane16;
    if (kh) {
        __builtin_amdgcn_s_setprio(L_PRIO);
        auto issue_dft = [&](int k) {
#pragma unroll
            for (int i = 0; i < 9; ++i) dft_piece(i, k);
        };
        LGKM_BAR(); TL_LOG(); issue_dft(3); issue_dft(4);
        unsigned wo1[5], wo2[5];
#pragma unroll
        for (int i = 0; i < 5; ++i) {
            const int cc = i * 64 + lane_here(), rho = cc / 10, ka = swap23(rho), col = (cc - rho * 10) * 16 + nt3 * 160;
            wo1[i] = (unsigned)((40 * qg + 20 * (ka >> 4) + (ka & 15)) * (D_ * 2) + col);
            wo2[i] = (unsigned)((160 * qg + 20 * (ka >> 2) + 16 + (ka & 3)) * (D_ * 2) + col);
        }
        const unsigned fo = OFF_FO + ((mt3 >> 2) * 32 + r32) * 64;
        _Pragma("unroll 1") for (int s = 2; s < 13; ++s) {
            VM_BAR(18);
            if (s == 3 && qg == 0) set_flag(ws, OFF_FO + (qb * 32 + head) * 64, tag);
            if (qg == 1) {
                if (s == 10) dma16c(lds0 + TOUCH_LDS, fo, rws, 0);
                if (s == 12 && !__all(*(volatile LDSP u64*)(lds0 + TOUCH_LDS + lane16) == tag)) poll2(ws, fo, fo, tag);
            }
            issue_dft(s + 3);
        }
        _Pragma("unroll 1") for (int j = 0; j < 4; ++j) {
            VM_BAR(18);
            if (j == 3) VM_BAR(0);
            const unsigned dst = lds0 + ((j + 2) & 3) * RSLOT;
#pragma unroll
            for (int i = 0; i < 4; ++i) dma16c(dst + (qg * 4 + i) * 1024, lane16, rws, OFF_OM + ((mt3 * 4 + i) * 16 + j * 4 + qg) * 1024);
#pragma unroll
            for (int i = 0; i < 5; ++i) dma16(dst + 16384 + qg * 5120 + i * 1024, wo1[i], rwo, j * 160 * (D_ * 2));
        }
#pragma unroll
        for (int i = 0; i < 4; ++i)
            dma16c(lds0 + 2 * RSLOT + (qg * 4 + i) * 1024, (unsigned)(hh * (S_ * 8) + r32 * 16), rws, OFF_OL + ((qg * 8 + i * 2) * S_ + mt3 * 64) * 8);
#pragma unroll
        for (int i = 0; i < 5; ++i) dma16(lds0 + 2 * RSLOT + 16384 + qg * 5120 + i * 1024, wo2[i], rwo, 0);
        const float gv = __uint_as_float(__builtin_amdgcn_raw_buffer_load_b32(rws, OFF_G + (mt3 * 64 + wt * 32 + r32) * 64, 0, 16));
        VM_BAR(0);
        { const unsigned fy = OFF_FY + ((mt3 * 8 + nt3) * 4 + (l & 3)) * 64; poll2(ws, fy, fy, tag); }
        STAMP(ws, 8);
        u32x4 yv[3][4];
#pragma unroll
        for (int e = 0; e < 3; ++e) {
            const unsigned yo = e == 2 && we ? OOB : OFF_Y + (unsigned)(((mt3 * 2 + wt) * 40 + nt3 * 5 + we * 3 + e) * 1024) + lane16;
#pragma unroll
            for (int p = 0; p < 4; ++p) yv[e][p] = ld16(rws, yo + p * (64 * 40 * 1024));
        }
        asm volatile("s_barrier" ::: "memory");
        TL_LOG();
        const float g1 = 1.f - gv;
#pragma unroll
        for (int e = 0; e < 3; ++e) {
            if (e == 2 && we) break;
            const unsigned za = e < 2 ? zx + e * 2048 : zx2;
            const f32x4 z0 = *(const LDSP f32x4*)(za), z1 = *(const LDSP f32x4*)(za + 1024);
            u32x4 v;
#pragma unroll
            for (int i = 0; i < 4; ++i) {
                const float y0 = (bf_lo(yv[e][0][i]) + bf_lo(yv[e][1][i])) + (bf_lo(yv[e][2][i]) + bf_lo(yv[e][3][i]));
                const float y1 = (bf_hi(yv[e][0][i]) + bf_hi(yv[e][1][i])) + (bf_hi(yv[e][2][i]) + bf_hi(yv[e][3][i]));
                const float za0 = i < 2 ? z0[2 * i] : z1[2 * i - 4], za1 = i < 2 ? z0[2 * i + 1] : z1[2 * i - 3];
                v[i] = pk(gv * y0 + g1 * za0, gv * y1 + g1 * za1);
            }
            *(u32x4*)((char*)out + (unsigned)((mt3 * 64 + wt * 32 + r32) * (D_ * 2) + (nt3 * 80 + (we * 3 + e) * 16 + hh * 8) * 2)) = v;
        }
        TL_LOG();
    } else {
        {
            const unsigned crd = lds0 + (wt * 64 + i16) * 128 + ((g ^ (i16 & 7)) << 4), ard = lds0 + 16384 + we * 5120 + lane16;
            f32x4 acc[5][4];
#pragma unroll
            for (int e = 0; e < 5; ++e)
#pragma unroll
                for (int t = 0; t < 4; ++t) acc[e][t] = f32x4{0.f, 0.f, 0.f, 0.f};
            bf16x8 cf[2][4], af[2][5];
            unsigned ca, aa;
            gemm_loop<2, 16, 10, 9, DFT_RPG, 3, true>(
                [&](unsigned so, int kk, int i) {
                    if (i == 0 && kk == 0) { aa = ard + so; PIN(aa); }
                    if (i == 1) { ca = (crd ^ (kk << 6)) + so; PIN(ca); }
                    if (i < 8 && (i & 1)) cf[kk][i >> 1] = lds_rd16(ca + (i >> 1) * 2048);
                    else af[kk][i >> 1] = lds_rd16(aa + kk * 10240 + (i >> 1) * 1024);
                },
                [&](int kk, int gq) {
                    const int e = gq >> 1;
#pragma unroll
                    for (int t = (gq & 1) * 2; t < (gq & 1) * 2 + 2; ++t) acc[e][t] = MFMA16(af[kk][e], cf[kk][t], acc[e][t]);
                });
            const unsigned ydst = OFF_Y + (unsigned)(((kq * 64 + mt * 4 + wt * 2) * 40 + ntile * 10 + we * 5) * 1024) + lane16;
#pragma unroll
            for (int e = 0; e < 5; ++e)
#pragma unroll
                for (int u = 0; u < 2; ++u) st16(rws, ydst + (u * 40 + e) * 1024, pack8x(acc[e][2 * u], acc[e][2 * u + 1]));
        }
        STAMP(ws, 7);
        const unsigned wrd = lds0 + 16384 + ((g >> 1) * 16 + (g & 1) * 4 + (i16 >> 2)) * 160 + 8 * (l & 3) + we * 96;
        f32x4 acc[3][2];
#pragma unroll
        for (int e = 0; e < 3; ++e) { acc[e][0] = f32x4{0.f, 0.f, 0.f, 0.f}; acc[e][1] = f32x4{0.f, 0.f, 0.f, 0.f}; }
        bf16x8 ob[2][2], wf[2][3];
        auto zrd = [&](int n, int i) {
            const unsigned so = (((n >> 2) + 2) & 3) * RSLOT;
            const int k = n & 3;
            if (i < 2) {
                if (n < 16) ob[n & 1][i] = lds_rd16(lds0 + so + (k * 4 + wt * 2 + i) * 1024 + lane16);
                else {
                    const unsigned a = lds0 + so + (k * 8 + 2 * g) * 512 + (wt * 32 + i * 16 + i16) * 8;
                    ob[n & 1][i] = __builtin_shufflevector(*(const LDSP s16x4*)(a), *(const LDSP s16x4*)(a + 512), 0, 1, 2, 3, 4, 5, 6, 7);
                }
            } else {
                const unsigned a = wrd + so + k * 5120 + (i == 4 && we ? 0 : (i - 2) * 32);
                wf[n & 1][i - 2] = lds_tr2(a, a + 1280);
            }
        };
#pragma unroll
        for (int i = 0; i < 5; ++i) zrd(0, i);
#pragma unroll
        for (int n = 0; n < 20; ++n) {
            if (n == 3 || n == 11) LGKM_BAR();
            if (n == Y_DR) { WAIT_VM(0); set_flag(ws, OFF_FY + (((mt * 2 + wt) * 8 + ntile * 2 + we) * 4 + kq) * 64, tag); }
            SB();
#pragma unroll
            for (int e = 0; e < 3; ++e) {
                if (n < 19) { zrd(n + 1, 2 * e); if (e < 2) zrd(n + 1, 2 * e + 1); }
                SB();
#pragma unroll
                for (int t = 0; t < 2; ++t) acc[e][t] = MFMA16(wf[n & 1][e], ob[n & 1][t], acc[e][t]);
                SB();
            }
        }
#pragma unroll
        for (int e = 0; e < 3; ++e) {
            f32x4 z0, z1;
#pragma unroll
            for (int i = 0; i < 4; ++i) {
                const auto r = __builtin_amdgcn_permlane16_swap(__float_as_uint(acc[e][0][i]), __float_as_uint(acc[e][1][i]), false, false);
                z0[i] = __uint_as_float(r[0]); z1[i] = __uint_as_float(r[1]);
            }
            const unsigned za = e < 2 ? zx + e * 2048 : zx2;
            *(LDSP f32x4*)(za) = z0; *(LDSP f32x4*)(za + 1024) = z1;
        }
        STAMP(ws, 10);
        LGKM_BAR();
    }
    STAMP(ws, 9);
}

extern "C" void kernel_launch(void* const* d_in, const int* in_sizes, int n_in, void* d_out, int out_size, void* d_ws,
                              size_t ws_size, hipStream_t stream) {
    (void)hipFuncSetAttribute((const void*)fused_kernel, hipFuncAttributeMaxDynamicSharedMemorySize, LDS_BYTES);
    fused_kernel<<<256, 512, LDS_BYTES, stream>>>((const bf16*)d_in[0], (const bf16*)d_in[1], (const bf16*)d_in[2], (const bf16*)d_in[3],
                                                  (const bf16*)d_in[4], (const bf16*)d_in[6], (const bf16*)d_in[7], (const bf16*)d_in[8],
                                                  (char*)d_ws, (bf16*)d_out);
}
```

```cpp
#include <hip/hip_runtime.h>
#include <hip/hip_bf16.h>
#include <stdint.h>

typedef short bf16x8 __attribute__((ext_vector_type(8)));
typedef short s16x4 __attribute__((ext_vector_type(4)));
typedef float f32x16 __attribute__((ext_vector_type(16)));
typedef float f32x32 __attribute__((ext_vector_type(32)));
typedef float f32x4 __attribute__((ext_vector_type(4)));
typedef float f32x2 __attribute__((ext_vector_type(2)));
typedef unsigned u32x4 __attribute__((ext_vector_type(4)));
typedef unsigned u32x2 __attribute__((ext_vector_type(2)));
typedef __bf16 bf16x2_t __attribute__((ext_vector_type(2)));
typedef unsigned long long u64;
typedef __attribute__((address_space(1))) u64 gu64;
typedef __hip_bfloat16 bf16;
#define LDSP __attribute__((address_space(3)))
#define DI __device__ __forceinline__
typedef __amdgpu_buffer_rsrc_t rsrc_t;

constexpr int S_ = 2048, D_ = 640, H_ = 32;
constexpr unsigned OFF_EPOCH = 0;
constexpr unsigned OFF_FQ = 16384, OFF_FAB = 32768, OFF_FO = 49152, OFF_FG = 65536, OFF_FY = 131072;
constexpr unsigned OFF_G = 262144;
constexpr unsigned OFF_Q1 = 524288;
constexpr unsigned OFF_Q2 = OFF_Q1 + H_ * 65536;
constexpr unsigned OFF_K2 = OFF_Q2 + 16 * 32768;
constexpr unsigned KV_CHUNK = 18432, KV_HEAD = 8 * KV_CHUNK;
constexpr unsigned OFF_KV = OFF_K2 + 16 * 32768;
constexpr unsigned OFF_AB = OFF_KV + H_ * KV_HEAD;
constexpr unsigned OFF_Y = OFF_AB + 4 * 64 * 20480;
constexpr unsigned OFF_OM = OFF_Y + 4 * S_ * D_ * 2;
constexpr unsigned OFF_OL = OFF_OM + S_ * 512 * 2;
constexpr unsigned OFF_WT = OFF_OL + H_ * S_ * 8;
constexpr unsigned WS_BYTES = OFF_WT + D_ * D_ * 2;
constexpr int LDS_BYTES = 163840;
constexpr int RSLOT = 36864;
constexpr int AT_SLOT = 23744;
constexpr int Q_LDS = 4 * RSLOT, TOUCH_LDS = Q_LDS + 12288;
constexpr unsigned SPIN_CAP = 1u << 19;
constexpr unsigned OOB = 0x7ffffff0u;

#define MFMA32(a, b, c) __builtin_amdgcn_mfma_f32_32x32x16_bf16((a), (b), (c), 0, 0, 0)
#define MFMA2B(a, b, c) __builtin_amdgcn_mfma_f32_32x32x4bf16_1k((a), (b), (c), 0, 0, 0)
#define LO16(v) __builtin_shufflevector(v, v, 0, 1, 2, 3, 4, 5, 6, 7, 8, 9, 10, 11, 12, 13, 14, 15)
#define HI16(v) __builtin_shufflevector(v, v, 16, 17, 18, 19, 20, 21, 22, 23, 24, 25, 26, 27, 28, 29, 30, 31)
#define MFMA16(a, b, c) __builtin_amdgcn_mfma_f32_16x16x32_bf16((a), (b), (c), 0, 0, 0)
#define WAIT_VM(n) asm volatile("s_waitcnt vmcnt(" #n ")" ::: "memory")
#define VM_BAR(n) asm volatile("s_waitcnt vmcnt(" #n ")\n\ts_barrier" ::: "memory")
#define LGKM_BAR() asm volatile("s_waitcnt lgkmcnt(0)\n\ts_barrier" ::: "memory")
#define EX2(v) __builtin_amdgcn_exp2f(v)
#define PIN(v) asm volatile("" : "+v"(v))
#define SB() __builtin_amdgcn_sched_barrier(0)
#define RLX_AGENT __ATOMIC_RELAXED, __HIP_MEMORY_SCOPE_AGENT
#define POLL_SLEEP 2
#define L_PRIO 1
#define DFT_RPG 2
#define PRIO_U 1
#define AB_RPG 2
#define QKV_DR 3
#define Y_DR 8
#define CODE_PF 44032

DI unsigned pk(float a, float b) { f32x2 v = {a, b}; return __builtin_bit_cast(unsigned, __builtin_convertvector(v, bf16x2_t)); }
DI float bf_lo(unsigned u) { return __uint_as_float(u << 16); }
DI float bf_hi(unsigned u) { return __uint_as_float(u & 0xffff0000u); }
DI rsrc_t mk_rsrc(const void* p, unsigned bytes) { return __builtin_amdgcn_make_buffer_rsrc((void*)p, (short)0, (int)bytes, 0x00020000); }
DI void dma16(unsigned lds_addr, unsigned voff, rsrc_t rs, unsigned soff) {
    asm volatile("s_mov_b32 m0, %0\n\ts_nop 0\n\tbuffer_load_dwordx4 %1, %2, %3 offen lds" ::"s"(lds_addr), "v"(voff), "s"(rs), "s"(soff) : "memory");
}
DI void dma16c(unsigned lds_addr, unsigned voff, rsrc_t rs, unsigned soff) {
    asm volatile("s_mov_b32 m0, %0\n\ts_nop 0\n\tbuffer_load_dwordx4 %1, %2, %3 offen sc1 lds" ::"s"(lds_addr), "v"(voff), "s"(rs), "s"(soff) : "memory");
}
DI void touch(unsigned lds_addr, unsigned voff, rsrc_t rs) {
    asm volatile("s_mov_b32 m0, %0\n\ts_nop 0\n\tbuffer_load_dword %1, %2, 0 offen sc1 lds" ::"s"(lds_addr), "v"(voff), "s"(rs) : "memory");
}
DI bf16x8 lds_rd16(unsigned a) { return *(const LDSP bf16x8*)(a); }
DI s16x4 lds_rd8(unsigned a) { return *(const LDSP s16x4*)(a); }
DI s16x4 lds_tr(unsigned a) { return __builtin_amdgcn_ds_read_tr16_b64_v4i16((LDSP s16x4*)(a)); }
DI bf16x8 lds_tr2(unsigned a0, unsigned a1) { return __builtin_shufflevector(lds_tr(a0), lds_tr(a1), 0, 1, 2, 3, 4, 5, 6, 7); }
DI void st16(rsrc_t rs, unsigned off, u32x4 v) { __builtin_amdgcn_raw_buffer_store_b128(v, rs, off, 0, 16); }
DI void st8(rsrc_t rs, unsigned off, u32x2 v) { __builtin_amdgcn_raw_buffer_store_b64(v, rs, off, 0, 16); }
DI void st4(rsrc_t rs, unsigned off, unsigned v) { __builtin_amdgcn_raw_buffer_store_b32(v, rs, off, 0, 16); }
DI u32x4 ld16(rsrc_t rs, unsigned off) { return __builtin_amdgcn_raw_buffer_load_b128(rs, off, 0, 16); }
DI int swap23(int i) { return (i & ~12) | ((i & 4) << 1) | ((i & 8) >> 1); }
DI u32x4 pack8(const f32x4& a, const f32x4& b) { return u32x4{pk(a[0], a[1]), pk(a[2], a[3]), pk(b[0], b[1]), pk(b[2], b[3])}; }
DI u32x4 pack8x(const f32x4& a, const f32x4& b) {
    const auto r0 = __builtin_amdgcn_permlane16_swap(pk(a[0], a[1]), pk(b[0], b[1]), false, false);
    const auto r1 = __builtin_amdgcn_permlane16_swap(pk(a[2], a[3]), pk(b[2], b[3]), false, false);
    return u32x4{r0[0], r1[0], r0[1], r1[1]};
}

DI u64 epoch_tag(unsigned lds_copy) {
    const u32x2 v = *(volatile LDSP u32x2*)(lds_copy);
    const u64 e = ((u64)(unsigned)__builtin_amdgcn_readfirstlane(v[1]) << 32) | (unsigned)__builtin_amdgcn_readfirstlane(v[0]);
    return e + 1 ? e + 1 : 1;
}
DI int lane_here() { int l = threadIdx.x & 63; PIN(l); return l; }
DI void set_flag(char* ws, unsigned off, u64 tag) {
    const int l = lane_here();
    if (l < 8) __hip_atomic_store((gu64*)(ws + off + l * 8), tag, RLX_AGENT);
}
DI void poll2(char* ws, unsigned off0, unsigned off1, u64 tag) {
    _Pragma("unroll 1") for (unsigned spins = 0; spins < SPIN_CAP; ++spins) {
        const u64 a = __hip_atomic_load((gu64*)(ws + off0), RLX_AGENT), b = __hip_atomic_load((gu64*)(ws + off1), RLX_AGENT);
        if (__all(a == tag && b == tag)) break;
        __builtin_amdgcn_s_sleep(POLL_SLEEP);
    }
    asm volatile("" ::: "memory");
}

template <int S0, int NS, int NG, int NR, int RPG, int DR, bool LASTBAR, class RD, class MM>
DI void gemm_loop(RD rd, MM mm) {
    unsigned so = (S0 & 3) * RSLOT;
#pragma unroll
    for (int i = 0; i < NR; ++i) rd(so, 0, i);
    auto half = [&](int kk, bool first) {
        SB();
#pragma unroll
        for (int gq = 0; gq < NG; ++gq) {
            mm(kk, gq, first); SB();
#pragma unroll
            for (int i = gq * RPG; i < (gq + 1) * RPG && i < NR; ++i) rd(so, kk ^ 1, i);
            SB();
        }
        __builtin_amdgcn_s_waitcnt(0xC07F);
    };
    __builtin_amdgcn_s_waitcnt(0xC07F);
    unsigned s1 = ((S0 + 1) & 3) * RSLOT, s2 = ((S0 + 2) & 3) * RSLOT, s3 = ((S0 + 3) & 3) * RSLOT;
    auto stage = [&](bool first, bool drain) {
        half(0, first);
        if (drain) WAIT_VM(0);
        asm volatile("s_barrier" ::: "memory");
        const unsigned t = so;
        so = s1; s1 = s2;
        if (S0 == 2) s2 = t; else { s2 = s3; s3 = t; }
        half(1, false);
    };
    constexpr int P = DR > 2 ? DR : 2;
    stage(true, false);
#pragma unroll
    for (int it = 2; it < P; ++it) stage(false, false);
    _Pragma("unroll 1") for (int it = P; it < NS; ++it) stage(false, DR > 0);
    half(0, false);
    if (LASTBAR) asm volatile("s_barrier" ::: "memory");
#pragma unroll
    for (int gq = 0; gq < NG; ++gq) mm(1, gq, false);
}

DI void ab_compute(bool isb, int m0, int n0, rsrc_t rws, unsigned lds0, int wv) {
    const int l = threadIdx.x & 63, i16 = l & 15, g = l >> 4;
    const unsigned xrd = lds0 + (wv * 32 + i16) * 128 + ((g ^ (i16 & 7)) << 4), wrd = lds0 + 16384 + i16 * 128 + ((g ^ (i16 & 7)) << 4);
    f32x4 acc[2][5];
    const f32x4 z4 = {0.f, 0.f, 0.f, 0.f};
    bf16x8 xf[2][2], wf[2][5];
    unsigned xa, wa;
    gemm_loop<10, 10, 5, 7, AB_RPG, QKV_DR, false>(
        [&](unsigned so, int kk, int i) {
            if (i == 0) { xa = (xrd ^ (kk << 6)) + so; PIN(xa); }
            if (i == 2) { wa = (wrd ^ (kk << 6)) + so; PIN(wa); }
            if (i < 2) xf[kk][i] = lds_rd16(xa + i * 2048);
            else wf[kk][i - 2] = lds_rd16(wa + (i - 2) * 2048);
        },
        [&](int kk, int j, bool first) {
            acc[0][j] = MFMA16(xf[kk][0], wf[kk][j], first ? +z4 : +acc[0][j]);
            acc[1][j] = MFMA16(xf[kk][1], wf[kk][j], first ? +z4 : +acc[1][j]);
        });
    const float sg = isb ? -1.f : 1.f;
    const int ntile = n0 / 160, e16 = (n0 - ntile * 160) >> 4, k32 = ((isb ? 2048 : 0) + m0 + 32 * wv) >> 5;
#pragma unroll
    for (int j = 0; j < 5; ++j)
        st16(rws, OFF_AB + (unsigned)(((ntile * 128 + k32) * 10 + e16 + j) * 1024 + l * 16), pack8(acc[0][j] * sg, acc[1][j] * sg));
}

template <int KIND>
DI void qkv_compute(int m0, int nt, rsrc_t rws, unsigned lds0, int wv) {
    const int l = threadIdx.x & 63, wm = wv & 1, wn = wv >> 1, r32 = l & 31, hh = l >> 5, q = (l >> 2) & 3, p = l & 3;
    const unsigned xrd = lds0 + (wm * 64 + r32) * 128 + ((hh ^ ((r32 >> 1) & 7)) << 4);
    const int pq = KIND == 1 ? ((p & 1) << 1) | (p >> 1) : p;
    const unsigned wrd = lds0 + 16384 + (8 * hh + q) * 256 + (((2 * wn) ^ q) << 6) + ((l >> 4) & 1) * 32 + 8 * pq;
    const bool isq = nt < 5;
    const int tile = (m0 + wm * 64) >> 5, pos16 = (isq ? swap23(r32) : r32) * 16;
    const unsigned b2 = (isq ? OFF_Q2 : OFF_K2) + tile * 512 + pos16, hstep = isq ? 65536 : KV_HEAD;
    const unsigned b1 = (isq ? OFF_Q1 + tile * 1024 : OFF_KV + (tile >> 3) * KV_CHUNK + (tile & 7) * 1024) + pos16;
    unsigned off[2][2][2];
#pragma unroll
    for (int j = 0; j < 2; ++j)
#pragma unroll
        for (int s2 = 0; s2 < 2; ++s2) {
            if (KIND == 2) {
                const int col = (nt - 10) * 128 + wn * 64 + 32 * j + 16 * s2 + (l & 15), s = m0 + wm * 64;
                const int h = col / 20, d = col - h * 20;
                const int vp = d - 8 * (d >= 8) - 2 * (d >= 18), vh = (0x30FF0 >> d) & 1;
                off[j][s2][0] = (nt >= 15 ? OOB - 0x2000000u : OFF_KV + 8192 + (s >> 8) * KV_CHUNK + (s & 255) * 2) +
                                (unsigned)(h * KV_HEAD + vp * 1024 + vh * 512 + ((l >> 4) & 1) * 32 + hh * 16);
                off[j][s2][1] = off[j][s2][0] + 64;
            } else {
                const int c8 = (isq ? nt : nt - 5) * 16 + wn * 8 + 4 * j + 2 * s2 + hh;
                const int pair = c8 / 5, oc = c8 - pair * 5, up = oc > 2;
                const unsigned o2 = b2 + pair * 32768, o1 = b1 + (2 * pair + up) * hstep + (oc - 3 * up) * 512;
                off[j][s2][0] = oc == 2 ? o2 : o1;
                off[j][s2][1] = off[j][s2][0] + (oc == 2 ? 512 : 1024);
            }
            PIN(off[j][s2][0]); PIN(off[j][s2][1]);
        }
    f32x16 acc[2][2];
    const f32x16 z16 = {0.f, 0.f, 0.f, 0.f, 0.f, 0.f, 0.f, 0.f, 0.f, 0.f, 0.f, 0.f, 0.f, 0.f, 0.f, 0.f};
    bf16x8 xf[2][2][2], wf[2][2][2];
    unsigned xa[2], wa[2];
    asm volatile("s_barrier" ::: "memory");
    gemm_loop<0, 10, 8, 8, 2, 0, true>(
        [&](unsigned so, int kk, int i) {
            const int k2 = i >> 2, j = i & 1;
            if (i == 0) { xa[0] = (xrd ^ (kk << 6)) + so; xa[1] = xa[0] ^ 32; wa[0] = wrd + so; wa[1] = wa[0] ^ 64; PIN(xa[0]); PIN(xa[1]); PIN(wa[0]); PIN(wa[1]); }
            if (i & 2) { const unsigned a = wa[j] + (kk * 2 + k2) * 4096; wf[kk][k2][j] = lds_tr2(a, a + 1024); }
            else xf[kk][k2][j] = lds_rd16(xa[k2] + j * 4096);
        },
        [&](int kk, int gq, bool first) {
            const int k2 = gq >> 2, m = (gq >> 1) & 1, j = gq & 1;
            const f32x16 c = first && !k2 ? +z16 : +acc[m][j];
            acc[m][j] = (KIND == 1) ? MFMA32(wf[kk][k2][j], xf[kk][k2][m], c) : MFMA32(xf[kk][k2][m], wf[kk][k2][j], c);
        });
    const float sc = isq ? 0.22360679774997896f * 1.4426950408889634f : 1.f;
#pragma unroll
    for (int m = 0; m < 2; ++m)
#pragma unroll
        for (int j = 0; j < 2; ++j) {
            const f32x16& a = acc[m][j];
            u32x4 lo, hi;
#pragma unroll
            for (int i = 0; i < 4; ++i) {
                if (KIND == 2) {
                    const auto r = __builtin_amdgcn_permlane16_swap(pk(a[2 * i], a[2 * i + 1]), pk(a[8 + 2 * i], a[9 + 2 * i]), false, false);
                    lo[i] = r[0]; hi[i] = r[1];
                } else {
                    lo[i] = pk(a[2 * i] * sc, a[2 * i + 1] * sc); hi[i] = pk(a[8 + 2 * i] * sc, a[9 + 2 * i] * sc);
                }
            }
            st16(rws, off[j][0][m], lo);
            st16(rws, off[j][1][m], hi);
        }
}

DI float half_swap_max(float m) {
    auto r = __builtin_amdgcn_permlane32_swap(__float_as_uint(m), __float_as_uint(m), false, false);
    return fmaxf(__uint_as_float(r[0]), __uint_as_float(r[1]));
}
DI float max16(const f32x16& s) {
    float a = fmaxf(fmaxf(s[0], s[1]), s[2]), b = fmaxf(fmaxf(s[3], s[4]), s[5]);
    a = fmaxf(fmaxf(a, s[6]), s[7]); b = fmaxf(fmaxf(b, s[8]), s[9]);
    a = fmaxf(fmaxf(a, s[10]), s[11]); b = fmaxf(fmaxf(b, s[12]), s[13]);
    a = fmaxf(fmaxf(a, s[14]), s[15]);
    return fmaxf(a, b);
}

__global__ __launch_bounds__(512, 2) void fused_kernel(const bf16* x, const bf16* cr, const bf16* ci, const bf16* wr, const bf16* wi,
                                                       const bf16* wqkv, const bf16* wo, const bf16* gw, char* ws, bf16* out) {
    extern __shared__ __attribute__((aligned(16))) char smem[];
    const unsigned lds0 = (unsigned)(uintptr_t)(LDSP char*)smem;
    const int b = blockIdx.x, xcd = b & 7, slot = b >> 3, l = threadIdx.x & 63;
    const int wv = __builtin_amdgcn_readfirstlane(threadIdx.x >> 6);
    const int mt1 = (xcd & 1) * 8 + (slot & 7), nt1 = (xcd >> 1) * 4 + (slot >> 3);
    const int head = xcd * 4 + (slot >> 3), qb = slot & 7, pair = head >> 1, odd = head & 1;
    const int kq = xcd & 3, mt = (xcd >> 2) * 8 + (slot >> 2), ntile = slot & 3;
    const int mt3 = xcd * 4 + (slot >> 3), nt3 = slot & 7;
    const rsrc_t rws = mk_rsrc(ws, WS_BYTES);
    const unsigned lane16 = l * 16;
    const int hh = l >> 5, r32 = l & 31, i16 = l & 15, g = l >> 4;
    const int qg = wv & 3, kh = wv >> 2;
    const unsigned swz = (unsigned)(((l & 7) ^ (l >> 3)) << 4);
    u64 tag;
    bool kv_all = true;
    const unsigned kvsrc = OFF_KV + head * KV_HEAD + qg * 1024, k2src = OFF_K2 + pair * 32768 + qg * 1024;
    auto kv_piece = [&](int i, int c, int sl) {
        const unsigned dst = lds0 + sl * AT_SLOT, src = kvsrc + c * KV_CHUNK;
        if (i < 2) dma16c(dst + qg * 1024 + i * 4096, lane16, rws, src + i * 4096);
        else if (i == 2) dma16c(dst + 8192 + qg * 1024, lane16, rws, k2src + c * 4096);
        else if (i < 5 || qg < 2) dma16c(dst + 12288 + (qg + (i - 3) * 4) * 1040, lane16, rws, src + 8192 + (i - 3) * 4096);
    };
    unsigned voff, koff, k2off, coff, tch_c, tch_w, tch_a, kvv2;
    auto setup = [&]() {
        const int vp = r32 - 4 * (r32 >= 4) - 4 * (r32 >= 12) - 4 * (r32 >= 18), vh = (0x30FF0 >> r32) & 1;
        const int vd = vp * 1040 + vh * 512, vc = 10928 - 528 * ((0x440000 >> r32) & 1);
        voff = 12288 + hh * 16 + kh * 256 + (((0x33FFFF >> r32) & 1) ? vd : vc);
        koff = lane16 + kh * 4096; k2off = 8192 + r32 * 16 + kh * 2048 + odd * 8;
        coff = (unsigned)((mt * 128 + 32 * qg + (l >> 3)) * 4096 + (kq & 1) * 2048) + swz;
        const unsigned l128 = l * 128, big = 0x70000000u;
        tch_c = (unsigned)((mt * 128 + ntile * 32 + wv * 4) * 4096 + (kq & 1) * 2048) + l128 + (l >= 16) * big;
        tch_w = OFF_WT + (unsigned)((slot * 8 + wv) * 25 * 128) + l128 + (l >= 25) * big;
        tch_a = OFF_AB + (unsigned)((ntile * 64 + kq * 16) * 20480 + ((mt & 7) * 8 + wv) * 40 * 128) + l128 + (l >= 14) * big;
        kvv2 = wv < 6 ? lane16 : OOB;
        PIN(voff); PIN(koff); PIN(k2off); PIN(coff); PIN(tch_c); PIN(tch_w); PIN(tch_a); PIN(kvv2);
    };
    const char* pc;
    asm volatile("s_getpc_b64 %0" : "=s"(pc));

    if (__builtin_expect(kh, 1)) {
        __builtin_amdgcn_s_setprio(L_PRIO);
        const rsrc_t rx = mk_rsrc(x, S_ * D_ * 2), rw = mk_rsrc(wqkv, D_ * 1920 * 2);
        const unsigned xoff = (unsigned)((mt1 * 128 + 32 * qg + 8 * hh + ((l >> 3) & 3)) * (D_ * 2)) + swz;
        const int k0 = (2 * (slot & 7) + (((slot >> 3) * 5 + 1) >> 1)) % 10;
        const unsigned xoffq = (unsigned)((mt1 * 128 + 64 * (qg >> 1) + 4 * (qg & 1) + 8 * hh + ((l >> 3) & 3)) * (D_ * 2) + (((l & 7) ^ (4 * (qg & 1) + g)) << 4));
        const unsigned woff = (unsigned)((16 * qg + g) * 3840 + (((i16 >> 2) ^ g) << 6) + ((i16 & 3) << 4) + (nt1 < 15 ? nt1 : 14) * 256);
        auto x_part = [&](int ks, unsigned dst) {
#pragma unroll
            for (int i = 0; i < 4; ++i) dma16(dst + (qg * 4 + i) * 1024, xoff, rx, ks * 128 + (16 * (i & 1) + 4 * (i >> 1)) * (D_ * 2));
        };
        auto issue_qkv = [&](int k) {
            const int ks = k0 + k - (k0 + k >= 10 ? 10 : 0);
            const unsigned dst = lds0 + (k & 3) * RSLOT;
#pragma unroll
            for (int i = 0; i < 4; ++i) dma16(dst + (8 * (qg >> 1) + (qg & 1) + 2 * i) * 1024, xoffq, rx, ks * 128 + 16 * i * (D_ * 2));
#pragma unroll
            for (int i = 0; i < 4; ++i) dma16(dst + 16384 + (qg * 4 + i) * 1024, woff, rw, (ks * 64 + 4 * i) * 3840);
        };
        if (qg == 0) touch(lds0 + TOUCH_LDS, l < 11 ? (unsigned)((l * 32 + slot) * 128) : OOB, mk_rsrc(pc, CODE_PF));
        issue_qkv(0); issue_qkv(1);
        const int f = b * 4 + qg, fn = f / 100, fk = (f - fn * 100) / 5, fe = f - fn * 100 - fk * 5, ka = l >> 1;
        const int frow_w = fk < 16 ? 40 * fk + 20 * (ka >> 4) + (ka & 15) : 160 * (fk - 16) + 20 * (ka >> 2) + 16 + (ka & 3);
        unsigned wtoff = f < 800 ? (unsigned)(frow_w * (D_ * 2) + (fn * 80 + fe * 16) * 2 + (l & 1) * 16) : OOB;
        PIN(wtoff);
        VM_BAR(8); issue_qkv(2); issue_qkv(3);
        const rsrc_t rd = mk_rsrc(nt1 >= 8 ? wi : wr, D_ * D_ * 2);
        const unsigned doff = (unsigned)(((nt1 & 7) * 80 + 24 * qg + (l >> 3)) * (D_ * 2)) + swz;
        const int n0 = (pair * 40) >> 7, n1 = (pair * 40 + 39) >> 7, sec = (l >> 2) & 3, sb5 = sec == 3 ? 0 : sec * 5;
        const int frow = hh ? (5 + (g & 1) * 5) * 16 + i16 : sb5 * 16 + ((qb * 2 + (sec == 1 || sec == 2 ? (l & 3) : (l & 1))) & 15);
        const unsigned fq0 = OFF_FQ + (n0 * 16 + frow) * 64, fq1 = OFF_FQ + (n1 * 16 + frow) * 64, look = lds0 + 2 * RSLOT + 34816;
        unsigned dv1 = qg == 3 ? OOB : doff, dv2 = dv1;
        const rsrc_t rd2 = mk_rsrc(qg == 3 ? (const void*)ws : (const void*)(nt1 >= 8 ? wi : wr), qg == 3 ? +WS_BYTES : D_ * D_ * 2u);
        auto issue_ab = [&](int k) {
            const unsigned dst = lds0 + ((k + 2) & 3) * RSLOT;
            x_part(k, dst);
            dma16(dst + 16384 + qg * 3072, doff, rd, k * 128);
            dma16c(qg == 3 ? look : dst + 16384 + qg * 3072 + 1024, dv1, rd2, qg == 3 ? 0 : k * 128 + 8 * (D_ * 2));
            dma16c(qg == 3 ? look + 1024 : dst + 16384 + qg * 3072 + 2048, dv2, rd2, qg == 3 ? 0 : k * 128 + 16 * (D_ * 2));
        };
        auto seen_lds = [&]() { return __ballot((*(volatile LDSP u64*)(look + lane16) == tag) & (*(volatile LDSP u64*)(look + 1024 + lane16) == tag)); };
        if (qg == 1) dma16c(lds0 + TOUCH_LDS + 3072, l ? OOB : OFF_EPOCH, rws, 0);
        touch(lds0 + TOUCH_LDS, l < 25 ? (unsigned)((((xcd >> 1) & 1) * 3200 + (slot * 4 + qg) * 25 + l) * 128) : OOB, rd);
        VM_BAR(17); issue_qkv(4);
        VM_BAR(17); issue_qkv(5);
        _Pragma("unroll 1") for (int s = 3; s < 7; ++s) { VM_BAR(16); issue_qkv(s + 3); }
        {
            const rsrc_t rg = mk_rsrc(gw, D_ * 2);
#pragma unroll
            for (int i = 0; i < 3; ++i) dma16(lds0 + Q_LDS + qg * 3072 + i * 1024, i < 2 || l < 32 ? lane16 : OOB, rx, (b * 8 + qg * 2) * (D_ * 2) + i * 1024);
            dma16(lds0 + TOUCH_LDS + 1024, lane16, rg, 0);
            dma16(lds0 + TOUCH_LDS + 2048, lane16 + 1024, rg, 0);
            dma16(lds0 + 3 * RSLOT + 32768 + qg * 1024, wtoff, mk_rsrc(wo, D_ * D_ * 2), 0);
        }
        VM_BAR(22); issue_ab(0);
        VM_BAR(21); issue_ab(1);
        VM_BAR(14); issue_ab(2);
        VM_BAR(14); issue_ab(3);
        {
            float gs = 0.f;
#pragma unroll
            for (int i = 0; i < 3; ++i) {
                const unsigned c = i < 2 || r32 < 16 ? (r32 + 32 * i) * 16 : 0;
                const u32x4 gx = __builtin_bit_cast(u32x4, lds_rd16(lds0 + Q_LDS + qg * 3072 + hh * 1280 + c));
                const u32x4 gg = __builtin_bit_cast(u32x4, lds_rd16(lds0 + TOUCH_LDS + 1024 + c));
                float t = 0.f;
#pragma unroll
                for (int j = 0; j < 4; ++j) t += bf_lo(gx[j]) * bf_lo(gg[j]) + bf_hi(gx[j]) * bf_hi(gg[j]);
                gs += i < 2 || r32 < 16 ? t : 0.f;
            }
#pragma unroll
            for (int off = 16; off > 0; off >>= 1) gs += __shfl_xor(gs, off);
            if (r32 < 16) st4(rws, OFF_G + (b * 8 + qg * 2 + hh) * 64 + r32 * 4, __float_as_uint(1.f / (1.f + __expf(-gs))));
        }
        setup();
        tag = epoch_tag(lds0 + TOUCH_LDS + 3072);
        {
            const unsigned a = lds0 + 3 * RSLOT + 32768 + qg * 1024 + (8 * g + (i16 >> 2)) * 32 + 8 * (l & 3);
            st16(rws, b < 200 ? OFF_WT + (b * 4 + qg) * 1024 + lane16 : OOB, __builtin_bit_cast(u32x4, lds_tr2(a, a + 128)));
        }
        _Pragma("unroll 1") for (int s = 11; s < 17; ++s) {
            VM_BAR(14);
            if (s == 10 + QKV_DR && qg == 0) set_flag(ws, OFF_FQ + (nt1 * 16 + mt1) * 64, tag);
            if (s == 16 && qg == 3) { dv1 = fq0; dv2 = fq1; }
            issue_ab(s - 7);
        }
        VM_BAR(14);
        VM_BAR(7);
        u64 seen = seen_lds();
        const bool early = (unsigned)seen == 0xffffffffu;
        if (!early) {
            VM_BAR(0);
            _Pragma("unroll 1") for (unsigned spins = 0; spins < SPIN_CAP; ++spins) {
                seen = seen_lds();
                if ((unsigned)seen == 0xffffffffu) break;
                if (qg == 3 && !hh) { dma16c(look, fq0, rws, 0); dma16c(look + 1024, fq1, rws, 0); WAIT_VM(0); }
                else __builtin_amdgcn_s_sleep(4);
            }
        }
        dma16c(lds0 + Q_LDS + qg * 2048, lane16, rws, OFF_Q1 + head * 65536 + (qb * 8 + qg * 2) * 1024);
        dma16c(lds0 + Q_LDS + qg * 2048 + 1024, lane16, rws, OFF_Q1 + head * 65536 + (qb * 8 + qg * 2 + 1) * 1024);
        dma16c(lds0 + Q_LDS + 8192 + qg * 1024, lane16, rws, OFF_Q2 + pair * 32768 + (qb * 4 + qg) * 1024);
#pragma unroll
        for (int i = 0; i < 6; ++i) kv_piece(i, qb, 1);
        if (early) VM_BAR(8);
#pragma unroll
        for (int i = 0; i < 6; ++i) kv_piece(i, (qb + 1) & 7, 2);
        kv_all = seen == ~0ull;
        if (qg < 3) {
            const unsigned c = hh ? 0u : 0x3F803F80u;
            *(LDSP u32x4*)(lds0 + qg * AT_SLOT + 12288 + 10400 + lane16 + hh * 16) = u32x4{c, c, c, c};
        }
        __builtin_amdgcn_s_setprio(0);
        asm volatile("s_waitcnt vmcnt(5) lgkmcnt(0)\n\ts_barrier" ::: "memory");
    } else {
        setup();
        if (nt1 < 10) qkv_compute<1>(mt1 * 128, nt1, rws, lds0, wv);
        else qkv_compute<2>(mt1 * 128, nt1, rws, lds0, wv);
        ab_compute(nt1 >= 8, mt1 * 128, (nt1 & 7) * 80, rws, lds0, wv);
        tag = epoch_tag(lds0 + TOUCH_LDS + 3072);
        asm volatile("s_barrier" ::: "memory");
    }

    const bf16* cmat = kq < 2 ? cr : ci;
    const rsrc_t rc = mk_rsrc(cmat, S_ * S_ * 2);
    const unsigned absrc = OFF_AB + (unsigned)((ntile * 64 + kq * 16) * 20480 + qg * 5120);
    auto dft_piece = [&](int i, int k, int rs) {
        const unsigned dst = lds0 + rs * RSLOT;
        if (i < 5) dma16c(dst + 16384 + (qg * 5 + i) * 1024, lane16, rws, absrc + k * 20480 + i * 1024);
        else dma16(dst + (qg * 4 + i - 5) * 1024, coff, rc, k * 128 + (i - 5) * 8 * 4096);
    };
    auto pre_piece = [&](int j, int k, int rs) {
        const int i = kh * 2 + (j & 1);
        if (j < 2) dma16c(lds0 + rs * RSLOT + 16384 + (qg * 5 + i) * 1024, lane16, rws, absrc + k * 20480 + i * 1024);
        else dma16(lds0 + rs * RSLOT + (qg * 4 + i) * 1024, coff, rc, k * 128 + i * 8 * 4096);
    };
    bf16x8 q1[2];
    q1[0] = lds_rd16(lds0 + Q_LDS + qg * 2048 + kh * 1024 + lane16); q1[1] = lds_rd16(lds0 + Q_LDS + qg * 2048 + (kh ^ 1) * 1024 + lane16);
    const s16x4 q2 = lds_rd8(lds0 + Q_LDS + 8192 + qg * 1024 + (lane16 ^ (kh * 512)) + odd * 8);
    const f32x16 zero16 = {0.f, 0.f, 0.f, 0.f, 0.f, 0.f, 0.f, 0.f, 0.f, 0.f, 0.f, 0.f, 0.f, 0.f, 0.f, 0.f};
    unsigned sb = lds0 + AT_SLOT;
    bf16x8 k1f, v0f, v1f;
    s16x4 k2f;
    f32x16 sc[2], sn[2], o[2];
    f32x32 negm;
    float m0[2];
    {
#pragma unroll
        for (int i = 0; i < 32; ++i) negm[i] = 0.f;
        const f32x32 t = MFMA2B(lds_rd8(sb + k2off), q2, negm);
        const bf16x8 k0f = lds_rd16(sb + koff);
        sc[0] = MFMA32(k0f, q1[0], LO16(t));
        sc[1] = MFMA32(k0f, q1[1], HI16(t));
        k1f = lds_rd16(sb + koff + 1024); k2f = lds_rd8(sb + k2off + 512);
        v0f = lds_rd16(sb + voff); v1f = v0f;
        m0[0] = half_swap_max(max16(sc[0]));
        m0[1] = half_swap_max(max16(sc[1]));
#pragma unroll
        for (int i = 0; i < 16; ++i) { negm[i] = -m0[0]; negm[16 + i] = -m0[1]; sc[0][i] -= m0[0]; sc[1][i] -= m0[1]; }
    }
    o[0] = zero16; o[1] = zero16;
    u32x4 p0, p1 = {0u, 0u, 0u, 0u};
    float e12 = 0.f, e13 = 0.f, e14 = 0.f, e15 = 0.f;
    const unsigned kvs0 = OFF_KV + head * KV_HEAD + wv * 1024;
    const unsigned kvs2 = wv < 2 ? kvs0 + 16384 : OFF_K2 + pair * 32768 + (wv - 2) * 1024, kvstep2 = wv < 2 ? KV_CHUNK : 4096;
    const unsigned kvd2 = wv < 2 ? 12288 + (8 + wv) * 1040 : 8192 + ((wv - 2) & 3) * 1024;
    unsigned sbn = lds0 + 2 * AT_SLOT, sbl = lds0;
    unsigned src0 = kvs0 + ((qb + 2) & 7) * KV_CHUNK, src2 = kvs2 + ((qb + 2) & 7) * kvstep2;
    touch(lds0 + TOUCH_LDS, tch_c, rc);
    tch_c += 4096;
    __builtin_amdgcn_s_setprio(1);
    auto flags2 = [&](int lh) {
        return lh < 16 ? OFF_FAB + (((kq >> 1) * 8 + ntile * 2 + (lh >> 3)) * 16 + (kq & 1) * 8 + (lh & 7)) * 64
                       : OFF_FG + (lh < 32 ? mt3 * 8 + (lh & 7) : nt3 * 25 + (lh < 57 ? lh - 32 : 0)) * 64;
    };
#pragma unroll
    for (int ph = -1; ph < 4; ++ph) {
    if (ph == 1 && wv == 4) {
        set_flag(ws, OFF_FAB + (nt1 * 16 + mt1) * 64, tag); set_flag(ws, OFF_FG + b * 64, tag);
        if (b == 0) {
            const int lh = lane_here();
            poll2(ws, OFF_FQ + lh * 64, OFF_FQ + (64 + lh) * 64, tag);
            poll2(ws, OFF_FQ + (128 + lh) * 64, OFF_FQ + (192 + lh) * 64, tag);
            set_flag(ws, OFF_EPOCH, tag);
        }
    }
    if (ph == 2 && wv == 4) dma16c(lds0 + TOUCH_LDS + 1024, flags2(lane_here()), rws, 0);
    _Pragma("unroll 1") for (int c = ph < 1 ? ph + 1 : (ph < 3 ? 3 * ph : 7); c < (ph < 0 ? 1 : (ph < 2 ? 3 * ph + 3 : ph + 5)); ++c) {
        const bool last = ph >= 2;
#pragma unroll
        for (int u = 0; u < 8; ++u) {
            const int T = u >> 1, X = u & 1, Xn = X ^ 1;
            if (u == 4) {
                if (ph < 0 && wv == 4 && !kv_all) {
                    const int lh = lane_here(), s5 = 5 + ((lh >> 4) & 1) * 5;
                    poll2(ws, OFF_FQ + ((s5 + ((pair * 40) >> 7)) * 16 + (lh & 15)) * 64, OFF_FQ + ((s5 + ((pair * 40 + 39) >> 7)) * 16 + (lh & 15)) * 64, tag);
                }
                if (ph == 2 && wv == 4) {
                    const int lh = lane_here();
                    WAIT_VM(0);
                    if (!__all(*(volatile LDSP u64*)(lds0 + TOUCH_LDS + 1024 + lh * 16) == tag)) { const unsigned fa = flags2(lh); poll2(ws, fa, fa, tag); }
                }
                if (ph < 0) { if (kh) WAIT_VM(1); VM_BAR(63); } else if (ph == 3) VM_BAR(63); else VM_BAR(1);
                __builtin_amdgcn_s_setprio(1);
            }
            if (u == PRIO_U || (ph == 3 && u == 4 + PRIO_U)) __builtin_amdgcn_s_setprio(0);
            float a0, a1, a2, a3, b0, b1, b2, b3;
            SB();
            if (!X) {
                const f32x32 t = MFMA2B(k2f, q2, negm);
                sn[0] = LO16(t); sn[1] = HI16(t);
                k2f = T < 2 ? lds_rd8(sb + k2off + (T + 2) * 512) : lds_rd8(sbn + k2off + (T - 2) * 512);
            } else sn[0] = MFMA32(k1f, q1[0], sn[0]);
            p1[2] = pk(e12, e13); p1[3] = pk(e14, e15); PIN(p1);
            a0 = EX2(sc[X][0]); a1 = EX2(sc[X][1]); a2 = EX2(sc[X][2]); a3 = EX2(sc[X][3]); PIN(a0); PIN(a1); PIN(a2); PIN(a3);
            SB();
            if (X) {
                sn[1] = MFMA32(k1f, q1[1], sn[1]);
                k1f = T < 2 ? lds_rd16(sb + koff + (T + 2) * 1024) : lds_rd16(sbn + koff + (T - 2) * 1024);
                p0[0] = pk(a0, a1); p0[1] = pk(a2, a3);
            }
            b0 = EX2(sc[X][4]); b1 = EX2(sc[X][5]); b2 = EX2(sc[X][6]); b3 = EX2(sc[X][7]); PIN(b0); PIN(b1); PIN(b2); PIN(b3);
            SB();
            if (!X) { p0[0] = pk(a0, a1); p0[1] = pk(a2, a3); SB(); }
            if (ph == 2 && u >= 4) pre_piece(u & 3, 1, 3);
            if (last && u == 4 && !kh) dft_piece(4, ph == 2 ? 0 : 2, ph == 2 ? 2 : 0);
            if (ph == 2 && u == 5 && !kh) dft_piece(4, 1, 3);
            o[Xn] = MFMA32(v1f, __builtin_bit_cast(bf16x8, p1), o[Xn]);
            if (!X) v1f = lds_rd16(sb + voff + T * 64 + 32);
            p0[2] = pk(b0, b1); p0[3] = pk(b2, b3); PIN(p0);
            a0 = EX2(sc[X][8]); a1 = EX2(sc[X][9]); a2 = EX2(sc[X][10]); a3 = EX2(sc[X][11]); PIN(a0); PIN(a1); PIN(a2); PIN(a3);
            SB();
            o[X] = MFMA32(v0f, __builtin_bit_cast(bf16x8, p0), o[X]);
            if (X) v0f = T < 3 ? lds_rd16(sb + voff + (T + 1) * 64) : lds_rd16(sbn + voff);
            e12 = EX2(sc[X][12]); e13 = EX2(sc[X][13]); e14 = EX2(sc[X][14]); e15 = EX2(sc[X][15]); PIN(e12); PIN(e13); PIN(e14); PIN(e15);
            SB();
            p1[0] = pk(a0, a1); p1[1] = pk(a2, a3);
            SB();
            if (!last) {
                if (u == 4) dma16c(sbl + wv * 1024, lane16, rws, src0);
                if (u == 5) dma16c(sbl + 12288 + wv * 1040, lane16, rws, src0 + 8192);
                if (u == 6) dma16c(wv < 6 ? sbl + kvd2 : lds0 + TOUCH_LDS, kvv2, rws, src2);
                if (u == 7) {
                    if (ph <= 0) { touch(lds0 + TOUCH_LDS, tch_c, rc); tch_c += 4096; }
                    else { touch(lds0 + TOUCH_LDS, tch_a, rws); tch_a += 14 * 128; }
                    const bool wrap = src0 == kvs0 + 7 * KV_CHUNK;
                    src0 = wrap ? kvs0 : src0 + KV_CHUNK; src2 = wrap ? kvs2 : src2 + kvstep2;
                }
            } else if (u >= 4) pre_piece(u & 3, ph == 2 ? 0 : 2, ph == 2 ? 2 : 0);
            else if (ph == 3 && u == 0) touch(lds0 + TOUCH_LDS, tch_w, rws);
            if (X) { sc[0] = sn[0]; sc[1] = sn[1]; }
        }
        const unsigned sbt = sb; sb = sbn; sbn = sbl; sbl = sbt;
    }
    }
    p1[2] = pk(e12, e13); p1[3] = pk(e14, e15);
    o[1] = MFMA32(v1f, __builtin_bit_cast(bf16x8, p1), o[1]);
    {
        f32x16 of;
        const f32x16 ox = o[1], om = o[0];
        const float mx = m0[1], mm = m0[0];
        auto xbuf = [&](int w) { return lds0 + lane16 + (w < 3 ? RSLOT + w * 3072 : (w < 7 ? Q_LDS + (w - 3) * 3072 : TOUCH_LDS + 1024)); };
        const unsigned xw = xbuf(wv), xr = xbuf(wv ^ 4);
        *(LDSP f32x4*)(xw) = f32x4{ox[0], ox[1], ox[2], ox[3]};
        *(LDSP f32x4*)(xw + 1024) = f32x4{ox[4], ox[5], ox[6], ox[7]};
        *(LDSP f32x4*)(xw + 2048) = f32x4{ox[8], ox[9], ox[10], mx};
        asm volatile("s_waitcnt vmcnt(4) lgkmcnt(0)\n\ts_barrier" ::: "memory");
        const f32x4 r0 = *(const LDSP f32x4*)(xr), r1 = *(const LDSP f32x4*)(xr + 1024), r2 = *(const LDSP f32x4*)(xr + 2048);
        const float mn = fmaxf(mm, r2[3]), fa = EX2(mm - mn), fb = EX2(r2[3] - mn);
#pragma unroll
        for (int i = 0; i < 4; ++i) { of[i] = om[i] * fa + r0[i] * fb; of[4 + i] = om[4 + i] * fa + r1[i] * fb; }
#pragma unroll
        for (int i = 0; i < 3; ++i) of[8 + i] = om[8 + i] * fa + r2[i] * fb;
        float asum = 0.f;
#pragma unroll
        for (int i = 0; i < 10; ++i) asum += fabsf(of[i]);
        const bool bad = !(om[10] <= 1.0e18f) || !(r2[2] <= 1.0e18f) || !(asum <= 1.0e37f);
        if (__builtin_expect(__any(bad), 0)) {
            const bf16x8 fq1 = q1[0];
            const u32x4 qr = ld16(rws, OFF_Q2 + pair * 32768 + ((qb * 8 + qg * 2 + kh) * 32 + r32) * 16);
            const bool qlo = !hh && !odd, qhi = !hh && odd;
            const bf16x8 fq2 = __builtin_bit_cast(bf16x8, u32x4{qlo ? qr[0] : 0u, qlo ? qr[1] : 0u, qhi ? qr[2] : 0u, qhi ? qr[3] : 0u});
            const int rr = lane_here() & 31;
            const bool vdata = rr < 18 || rr == 20 || rr == 21, vone = rr == 18 || rr == 22;
            const int vp = rr < 4 ? rr : (rr < 12 ? rr - 4 : (rr < 18 ? rr - 8 : rr - 12)), vh = (rr >= 4 && rr < 12) || rr == 16 || rr == 17;
            const unsigned vg = 8192 + hh * 16 + (vdata ? vp * 1024 + vh * 512 : 0), vc = vone ? 0x3F803F80u : 0u;
            const u32x4 vconst = {vc, vc, vc, vc};
            float m = -INFINITY;
            of = zero16;
            _Pragma("unroll 1") for (int t = 0; t < 64; ++t) {
                const unsigned cb = OFF_KV + head * KV_HEAD + (t >> 3) * KV_CHUNK;
                const int T = t & 7;
                f32x16 s = MFMA32(__builtin_bit_cast(bf16x8, ld16(rws, cb + T * 1024 + lane16)), fq1, zero16);
                s = MFMA32(__builtin_bit_cast(bf16x8, ld16(rws, OFF_K2 + pair * 32768 + (t * 32 + r32) * 16)), fq2, s);
                const float mq = fmaxf(m, half_swap_max(max16(s)));
                const float al = EX2(m - mq);
                m = mq;
#pragma unroll
                for (int i = 0; i < 16; ++i) { of[i] *= al; s[i] = EX2(s[i] - mq); }
                const u32x4 pa = {pk(s[0], s[1]), pk(s[2], s[3]), pk(s[4], s[5]), pk(s[6], s[7])};
                const u32x4 pb = {pk(s[8], s[9]), pk(s[10], s[11]), pk(s[12], s[13]), pk(s[14], s[15])};
                const u32x4 va = ld16(rws, cb + vg + T * 64), vb2 = ld16(rws, cb + vg + T * 64 + 32);
                of = MFMA32(__builtin_bit_cast(bf16x8, vdata ? va : vconst), __builtin_bit_cast(bf16x8, pa), of);
                of = MFMA32(__builtin_bit_cast(bf16x8, vdata ? vb2 : vconst), __builtin_bit_cast(bf16x8, pb), of);
            }
        }
        const float rl = __builtin_amdgcn_rcpf(of[10]);
        const int q = (qb * 8 + qg * 2 + kh) * 32 + r32;
        st16(rws, OFF_OM + ((q >> 4) * 16 + pair) * 1024 + (odd * 2 + hh) * 256 + (q & 15) * 16,
             u32x4{pk(of[0] * rl, of[1] * rl), pk(of[2] * rl, of[3] * rl), pk(of[4] * rl, of[5] * rl), pk(of[6] * rl, of[7] * rl)});
        const unsigned xl = pk(of[8] * rl, of[9] * rl);
        const auto rx = __builtin_amdgcn_permlane32_swap(xl, xl, false, false);
        if (!hh) st8(rws, OFF_OL + (head * S_ + q) * 8, u32x2{rx[0], rx[1]});
    }

    const int wt = qg & 1, we = qg >> 1;
    if (kh) {
        __builtin_amdgcn_s_setprio(L_PRIO);
        auto z_o = [&](int j, int rs) {
#pragma unroll
            for (int i = 0; i < 4; ++i) dma16c(lds0 + rs * RSLOT + (qg * 4 + i) * 1024, lane16, rws, OFF_OM + ((mt3 * 4 + i) * 16 + j * 4 + qg) * 1024);
        };
        auto z_w = [&](int j, int rs) {
#pragma unroll
            for (int i = 0; i < 5; ++i) dma16c(lds0 + rs * RSLOT + 16384 + (qg * 5 + i) * 1024, lane16, rws, OFF_WT + ((nt3 * 20 + j * 4 + qg) * 5 + i) * 1024);
        };
        const unsigned fo = OFF_FO + ((mt3 >> 2) * 32 + r32) * 64;
        asm volatile("s_waitcnt lgkmcnt(0)" ::: "memory");
        auto issue_dft = [&](int k, int rs) {
#pragma unroll
            for (int i = 0; i < 9; ++i) dft_piece(i, k, rs);
        };
        VM_BAR(63); issue_dft(3, 2);
        int r0 = 3, r1 = 0, r2 = 2, ev = qg == 0 ? 0x5 : (qg == 1 ? 0xA04 : 0x4);
        _Pragma("unroll 1") for (int s = 2; s < 14; ++s) {
            VM_BAR(9);
            issue_dft(s + 2, r0);
            const int t = r0; r0 = r1; r1 = r2; r2 = t;
            if (ev & 1) {
                if (s == 2) set_flag(ws, OFF_FO + (qb * 32 + head) * 64, tag);
                else if (s == 4) z_w(3, 1);
                else if (s == 11) dma16c(lds0 + TOUCH_LDS, fo, rws, 0);
                else if (!__all(*(volatile LDSP u64*)(lds0 + TOUCH_LDS + lane16) == tag)) poll2(ws, fo, fo, tag);
            }
            ev >>= 1;
        }
        VM_BAR(9); z_o(3, 1); z_o(0, 3); z_w(0, 3);
        VM_BAR(13); z_o(1, 0); z_w(1, 0);
        VM_BAR(9); z_o(2, 2); z_w(2, 2);
        VM_BAR(0);
#pragma unroll
        for (int i = 0; i < 4; ++i)
            dma16c(lds0 + 3 * RSLOT + (qg * 4 + i) * 1024, (unsigned)(hh * (S_ * 8) + r32 * 16), rws, OFF_OL + ((qg * 8 + i * 2) * S_ + mt3 * 64) * 8);
        z_w(4, 3);
        VM_BAR(0);
        { const unsigned fy = OFF_FY + ((mt3 * 8 + nt3) * 4 + qg) * 64; poll2(ws, fy, fy, tag); }
#pragma unroll
        for (int i = 0; i < 10; ++i)
            dma16c(lds0 + (qg * 10 + i) * 1024, lane16, rws, OFF_Y + (unsigned)(((qg * 64 + mt3 * 2 + i / 5) * 40 + nt3 * 5 + i % 5) * 1024));
        VM_BAR(0);
    } else {
        const float gv = __uint_as_float(__builtin_amdgcn_raw_buffer_load_b32(rws, OFF_G + (mt3 * 64 + wt * 32 + r32) * 64, 0, 16));
        {
            const unsigned crd = lds0 + (wt * 64 + i16) * 128 + ((g ^ (i16 & 7)) << 4), ard = lds0 + 16384 + we * 5120 + lane16;
            f32x4 acc[5][4];
            const f32x4 z4 = {0.f, 0.f, 0.f, 0.f};
            bf16x8 cf[2][4], af[2][5];
            unsigned ca, aa;
            gemm_loop<2, 16, 10, 9, DFT_RPG, 2, true>(
                [&](unsigned so, int kk, int i) {
                    if (i == 0 && kk == 0) { aa = ard + so; PIN(aa); }
                    if (i == 1) { ca = (crd ^ (kk << 6)) + so; PIN(ca); }
                    if (i < 8 && (i & 1)) cf[kk][i >> 1] = lds_rd16(ca + (i >> 1) * 2048);
                    else af[kk][i >> 1] = lds_rd16(aa + kk * 10240 + (i >> 1) * 1024);
                },
                [&](int kk, int gq, bool first) {
                    const int e = gq >> 1;
#pragma unroll
                    for (int n = (gq & 1) * 2; n < (gq & 1) * 2 + 2; ++n) {
                        const int t = (e & 1) ? 3 - n : n;
                        acc[e][t] = MFMA16(af[kk][e], cf[kk][t], first ? +z4 : +acc[e][t]);
                    }
                });
            const unsigned ydst = OFF_Y + (unsigned)(((kq * 64 + mt * 4 + wt * 2) * 40 + ntile * 10 + we * 5) * 1024) + lane16;
#pragma unroll
            for (int e = 0; e < 5; ++e)
#pragma unroll
                for (int u = 0; u < 2; ++u) st16(rws, ydst + (u * 40 + e) * 1024, pack8x(acc[e][2 * u], acc[e][2 * u + 1]));
        }
        const unsigned wrd = lds0 + 16384 + we * 3072 + lane16;
        f32x4 acc[3][2];
#pragma unroll
        for (int e = 0; e < 3; ++e) { acc[e][0] = f32x4{0.f, 0.f, 0.f, 0.f}; acc[e][1] = f32x4{0.f, 0.f, 0.f, 0.f}; }
        bf16x8 ob[2][2], wf[2][3];
        auto zrd = [&](int n, int i) {
            const unsigned so = ((0x31203 >> (n & ~3)) & 3) * RSLOT;
            const int k = n & 3;
            if (i < 2) {
                if (n < 16) ob[n & 1][i] = lds_rd16(lds0 + so + (k * 4 + wt * 2 + i) * 1024 + lane16);
                else {
                    const unsigned a = lds0 + so + (k * 8 + 2 * g) * 512 + (wt * 32 + i * 16 + i16) * 8;
                    ob[n & 1][i] = __builtin_shufflevector(*(const LDSP s16x4*)(a), *(const LDSP s16x4*)(a + 512), 0, 1, 2, 3, 4, 5, 6, 7);
                }
            } else {
                wf[n & 1][i - 2] = lds_rd16(wrd + so + k * 5120 + (i - 2) * 1024);
            }
        };
#pragma unroll
        for (int i = 0; i < 5; ++i) zrd(0, i);
#pragma unroll
        for (int n = 0; n < 20; ++n) {
            if (n == Y_DR) { WAIT_VM(0); set_flag(ws, OFF_FY + (((mt * 2 + wt) * 8 + ntile * 2 + we) * 4 + kq) * 64, tag); }
            if (n == 3 || n == 15) LGKM_BAR();
            SB();
#pragma unroll
            for (int e = 0; e < 3; ++e) {
                if (n < 19) { zrd(n + 1, 2 * e); if (e < 2) zrd(n + 1, 2 * e + 1); }
                SB();
#pragma unroll
                for (int t = 0; t < 2; ++t) acc[e][t] = MFMA16(wf[n & 1][e], ob[n & 1][t], acc[e][t]);
                SB();
            }
        }
        float z[3][8];
#pragma unroll
        for (int e = 0; e < 3; ++e)
#pragma unroll
            for (int i = 0; i < 4; ++i) {
                const auto r = __builtin_amdgcn_permlane16_swap(__float_as_uint(acc[e][0][i]), __float_as_uint(acc[e][1][i]), false, false);
                z[e][i] = (1.f - gv) * __uint_as_float(r[0]); z[e][4 + i] = (1.f - gv) * __uint_as_float(r[1]);
                PIN(z[e][i]); PIN(z[e][4 + i]);
            }
        unsigned yrd = lds0 + (wt * 5 + we * 3) * 1024 + lane16;
        unsigned op = (unsigned)((mt3 * 64 + wt * 32 + r32) * (D_ * 2) + (nt3 * 80 + we * 48 + hh * 8) * 2);
        PIN(yrd); PIN(op);
        asm volatile("s_barrier" ::: "memory");
#pragma unroll
        for (int e = 0; e < 3; ++e) {
            if (e == 2 && we) break;
            u32x4 yv[4], v;
#pragma unroll
            for (int p = 0; p < 4; ++p) yv[p] = __builtin_bit_cast(u32x4, lds_rd16(yrd + (p * 10 + e) * 1024));
#pragma unroll
            for (int i = 0; i < 4; ++i) {
                const float y0 = (bf_lo(yv[0][i]) + bf_lo(yv[1][i])) + (bf_lo(yv[2][i]) + bf_lo(yv[3][i]));
                const float y1 = (bf_hi(yv[0][i]) + bf_hi(yv[1][i])) + (bf_hi(yv[2][i]) + bf_hi(yv[3][i]));
                v[i] = pk(gv * y0 + z[e][2 * i], gv * y1 + z[e][2 * i + 1]);
            }
            *(u32x4*)((char*)out + op + e * 32) = v;
        }
    }
}

extern "C" void kernel_launch(void* const* d_in, const int* in_sizes, int n_in, void* d_out, int out_size, void* d_ws,
                              size_t ws_size, hipStream_t stream) {
    (void)hipFuncSetAttribute((const void*)fused_kernel, hipFuncAttributeMaxDynamicSharedMemorySize, LDS_BYTES);
    fused_kernel<<<256, 512, LDS_BYTES, stream>>>((const bf16*)d_in[0], (const bf16*)d_in[1], (const bf16*)d_in[2], (const bf16*)d_in[3],
                                                  (const bf16*)d_in[4], (const bf16*)d_in[6], (const bf16*)d_in[7], (const bf16*)d_in[8],
                                                  (char*)d_ws, (bf16*)d_out);
}
```
